```python
import torch

def _softmax(x, axis=-1):
    return torch.softmax(x, dim=axis)
def _gelu(x):
    return torch.nn.functional.gelu(x, approximate="tanh")
def _silu(x):
    return torch.nn.functional.silu(x)
def _sigmoid(x):
    return torch.sigmoid(x)
def _layernorm(x, axis=-1, eps=1e-5):
    m = x.mean(dim=axis, keepdim=True)
    v = x.var(dim=axis, keepdim=True, unbiased=False)
    return (x - m) / torch.sqrt(v + eps)
def _scatter_add(tgt, idx, src, axis=0):
    return tgt.clone().index_add_(axis, idx, src)
def _segment_sum(src, seg, n):
    out = torch.zeros((n,) + src.shape[1:], dtype=src.dtype)
    return out.index_add_(0, seg, src)
def _pad(x, pw):
    flat = tuple(v for pair in reversed(pw) for v in pair)
    return torch.nn.functional.pad(x, flat)

kernel_name = 'interleave_layers__sliding_block__mixer_channel_mlp__prompt'

def setup_inputs():

    _w = (lambda a: (lambda t: t.to(torch.bfloat16) if t.is_floating_point() else t)(torch.as_tensor(a)))
    def _A_setup():
        B, S, D = 1, 2048, 640
        return {
            "x": _w((lambda *s: torch.randn(*s, dtype=torch.bfloat16) * 0.05)(B, S, D)),
            "wqkv": _w((lambda *s: torch.randn(*s, dtype=torch.bfloat16) * 0.05)(D, 3 * D)),
            "wo": _w((lambda *s: torch.randn(*s, dtype=torch.bfloat16) * 0.05)(D, D)),
        }
    _A_in = _A_setup()

    def _B_setup():
        B, S, D, F = 1, 2048, 640, 1728
        return {
            "x": _w((lambda *s: torch.randn(*s, dtype=torch.bfloat16) * 0.05)(B, S, D)),
            "w1": _w((lambda *s: torch.randn(*s, dtype=torch.bfloat16) * 1.6)(D, F) * (D ** -0.5)),
            "b1": _w((lambda *s: torch.randn(*s, dtype=torch.bfloat16) * 1.6)(F)),
            "w2": _w((lambda *s: torch.randn(*s, dtype=torch.bfloat16) * 0.4)(F, D) * (F ** -0.5)),
            "b2": _w((lambda *s: torch.randn(*s, dtype=torch.bfloat16) * 0.4)(D)),
        }
    _B_in = _B_setup()

    return {
        "A_x": _A_in["x"],
        "A_wqkv": _A_in["wqkv"],
        "A_wo": _A_in["wo"],
        "B_x": _B_in["x"],
        "B_w1": _B_in["w1"],
        "B_b1": _B_in["b1"],
        "B_w2": _B_in["w2"],
        "B_b2": _B_in["b2"],
    }

def reference(A_x, A_wqkv, A_wo, B_x, B_w1, B_b1, B_w2, B_b2):
    def _A_ref(x, wqkv, wo):
        B, S, D = x.shape
        H = 32; Dh = D // H; W = 256
        qkv = (lambda x, *d: x.permute(*d))((x @ wqkv).reshape(B, S, 3, H, Dh), 2, 0, 3, 1, 4)
        q, k, v = qkv[0], qkv[1], qkv[2]
        scores = torch.einsum("bhqd,bhkd->bhqk", q, k) * (Dh ** -0.5)
        idx = torch.arange(S)
        mask = (idx[:, None] - idx[None, :] >= 0) & (idx[:, None] - idx[None, :] < W)
        scores = torch.where(mask[None, None], scores, -1e9)
        p = _softmax(scores, axis=-1)
        o = torch.einsum("bhqk,bhkd->bhqd", p, v)
        return (lambda x, *d: x.permute(*d))(o, 0, 2, 1, 3).reshape(B, S, D) @ wo

    def _B_ref(x, w1, b1, w2, b2):
        y = _layernorm(x, axis=-1)
        h = _gelu(torch.einsum("bsd,df->bsf", y, w1) + b1)
        return torch.einsum("bsf,fd->bsd", h, w2) + b2

    x = A_x
    x = x + _A_ref(_layernorm(x, axis=-1), A_wqkv, A_wo)
    x = x + _B_ref(_layernorm(x, axis=-1), B_w1, B_b1, B_w2, B_b2)
    x = x + _A_ref(_layernorm(x, axis=-1), A_wqkv, A_wo)
    x = x + _B_ref(_layernorm(x, axis=-1), B_w1, B_b1, B_w2, B_b2)
    return x
```

```cpp
#include <hip/hip_runtime.h>
#include <hip/hip_bf16.h>
#include <stdint.h>

#define LAS __attribute__((address_space(3)))
#define DI __device__ __forceinline__
typedef unsigned u32;
typedef unsigned short u16;
typedef unsigned long long u64;
typedef unsigned u32x2 __attribute__((ext_vector_type(2)));
typedef unsigned u32x4 __attribute__((ext_vector_type(4)));
typedef float f32x2 __attribute__((ext_vector_type(2)));
typedef float f32x4 __attribute__((ext_vector_type(4)));
typedef __bf16 bf16x2 __attribute__((ext_vector_type(2)));
typedef __bf16 bf16x8 __attribute__((ext_vector_type(8)));
typedef __amdgpu_buffer_rsrc_t rsrc_t;

#define STOP_AFTER 99

constexpr u32 XT = 80 * 512;
constexpr u32 HT = 216 * 512;
constexpr u32 KVG = 7168;
constexpr u32 WS_E = 0;
constexpr u32 WS_FROW = 4096;
constexpr u32 WS_FCOL = WS_FROW + 2048;
constexpr u32 WS_KVF = WS_FCOL + 2048;
constexpr u32 WS_PH = WS_KVF + 4096;
constexpr u32 WS_ST = 65536;
constexpr u32 WS_CSQ = WS_ST + 4 * 131072;
constexpr u32 WS_CS1 = WS_CSQ + 16 * 20 * 512;
constexpr u32 WS_PQ = WS_CS1 + 16 * 20 * 512;
constexpr u32 WS_P1 = WS_PQ + 16 * 163840;
constexpr u32 WS_PO = WS_P1 + 16 * 143360;
constexpr u32 WS_P2 = WS_PO + 8 * 102400;
constexpr u32 WS_X0 = WS_P2 + 8 * 276480;
constexpr u32 WS_X1 = WS_X0 + 2 * 64 * XT;
constexpr u32 WS_O = WS_X1 + 2 * 64 * XT;
constexpr u32 WS_H = WS_O + 2 * 64 * XT;
constexpr u32 WS_KV = WS_H + 2 * 64 * HT;
constexpr u32 WS_DBG = WS_KV + 2 * 256 * 4 * KVG;
constexpr u32 WS_END = WS_DBG + 65536;
enum { PH_O = 0, PH_X1 = 1, PH_H = 2, PH_X2 = 3 };

constexpr u32 L_KV = 0;
constexpr u32 L_Q = 12 * KVG;
constexpr u32 L_EX = 122880;
constexpr u32 L_MISC = 153600;
constexpr u32 L_SA = L_MISC, L_SB = L_MISC + 512, L_CSQ = L_MISC + 1024, L_CS1 = L_MISC + 1536, L_B1 = L_MISC + 2048, L_B2 = L_MISC + 2560,
              L_CNT = L_MISC + 3072;
constexpr u32 LDS_BYTES = L_MISC + 4096;

template <int I> struct IC { static constexpr int v = I; };
template <int B, int E, class F> DI void sfor(F&& f) { if constexpr (B < E) { f(IC<B>{}); sfor<B + 1, E>(f); } }
DI u32 rfl(u32 x) { return __builtin_amdgcn_readfirstlane(x); }
DI f32x4 mfma(u32x4 a, u32x4 b, f32x4 c) {
    return __builtin_amdgcn_mfma_f32_16x16x32_bf16(__builtin_bit_cast(bf16x8, a), __builtin_bit_cast(bf16x8, b), c, 0, 0, 0);
}
DI u32 pk(float a, float b) { f32x2 v = {a, b}; return __builtin_bit_cast(u32, __builtin_convertvector(v, bf16x2)); }
DI float bflo(u32 w) { return __builtin_bit_cast(float, w << 16); }
DI float bfhi(u32 w) { return __builtin_bit_cast(float, w & 0xffff0000u); }
DI float f_of(u32 w) { return __builtin_bit_cast(float, w); }
DI u32 u_of(float f) { return __builtin_bit_cast(u32, f); }
#define SB() __builtin_amdgcn_sched_barrier(0)
#define BAR() asm volatile("s_barrier" ::: "memory")
#define LBAR() asm volatile("s_waitcnt lgkmcnt(0)\n\ts_barrier" ::: "memory")
#define PHASE_VARS() \
    int lane = lane_o, c = c_o, j = j_o, wid = wid_o; \
    asm volatile("" : "+v"(lane), "+s"(c), "+s"(j), "+s"(wid)); \
    const int t = lane & 15, g = lane >> 4, jj = j & 7, hf = j >> 3, dw = wid - 5; const u32 vl = lane * 16; \
    const u32 xin = WS_X0 + l * 64 * XT, ph = WS_PH + l * 4 * 2048 + c * 128; \
    (void)t; (void)g; (void)jj; (void)hf; (void)dw; (void)vl; (void)xin; (void)ph;
#define WAITV(n) asm volatile("s_waitcnt vmcnt(%0)" :: "n"(n) : "memory")
DI void dma16(rsrc_t rs, u32 voff, u32 soff, u32 ldsaddr) {
    asm volatile("s_mov_b32 m0, %0\n\ts_nop 0\n\tbuffer_load_dwordx4 %1, %2, %3 offen sc1 lds" :: "s"(ldsaddr), "v"(voff), "s"(rs), "s"(soff) : "memory");
}
template <int OFF> DI void gload(u32x4& d, rsrc_t rs, u32 voff, u32 soff) {
    asm volatile("buffer_load_dwordx4 %0, %1, %2, %3 offen offset:%4 sc1" : "=v"(d) : "v"(voff), "s"(rs), "s"(soff), "n"(OFF));
}
template <int OFF> DI void lread(u32x4& d, u32 addr) { asm volatile("ds_read_b128 %0, %1 offset:%2" : "=v"(d) : "v"(addr), "n"(OFF)); }
DI void st16(rsrc_t rs, u32 voff, u32 soff, u32x4 v) { __builtin_amdgcn_raw_buffer_store_b128(v, rs, voff, soff, 16); }
DI void st8(rsrc_t rs, u32 voff, u32 soff, u32x2 v) { __builtin_amdgcn_raw_buffer_store_b64(v, rs, voff, soff, 16); }
DI void st4(rsrc_t rs, u32 voff, u32 soff, u32 v) { __builtin_amdgcn_raw_buffer_store_b32(v, rs, voff, soff, 16); }
DI u32x4 ld16(rsrc_t rs, u32 voff, u32 soff) { return __builtin_amdgcn_raw_buffer_load_b128(rs, voff, soff, 16); }
DI u32x2 ld8(rsrc_t rs, u32 voff, u32 soff) { return __builtin_amdgcn_raw_buffer_load_b64(rs, voff, soff, 16); }
template <class T> DI T lds_ld(u32 a) { return *(const LAS T*)(uintptr_t)a; }
template <class T> DI void lds_st(u32 a, T v) { *(LAS T*)(uintptr_t)a = v; }
DI float rows_sum(float v) {
    auto a = __builtin_amdgcn_permlane32_swap(u_of(v), u_of(v), false, false); v = f_of(a[0]) + f_of(a[1]);
    auto b = __builtin_amdgcn_permlane16_swap(u_of(v), u_of(v), false, false); return f_of(b[0]) + f_of(b[1]);
}
DI float rows_max(float v) {
    auto a = __builtin_amdgcn_permlane32_swap(u_of(v), u_of(v), false, false); v = fmaxf(f_of(a[0]), f_of(a[1]));
    auto b = __builtin_amdgcn_permlane16_swap(u_of(v), u_of(v), false, false); return fmaxf(f_of(b[0]), f_of(b[1]));
}
DI u32x4 swap_pair(u32x2 p, u32x2 q) {
    auto a = __builtin_amdgcn_permlane16_swap(p.x, q.x, false, false);
    auto b = __builtin_amdgcn_permlane16_swap(p.y, q.y, false, false);
    return u32x4{a[0], b[0], a[1], b[1]};
}
DI u32x2 pk4(f32x4 v) { return u32x2{pk(v[0], v[1]), pk(v[2], v[3])}; }

DI void poll(rsrc_t rs, u32 voff, u64 tag, int& dead) {
    if (dead) return;
    for (int it = 0; it < (1 << 19); ++it) {
        u32x2 v;
        asm volatile("buffer_load_dwordx2 %0, %1, %2, 0 offen sc1\n\ts_waitcnt vmcnt(0)" : "=v"(v) : "v"(voff), "s"(rs) : "memory");
        if (__builtin_amdgcn_ballot_w64(v.x != (u32)tag || v.y != (u32)(tag >> 32)) == 0) return;
        __builtin_amdgcn_s_sleep(1);
    }
    dead = 1;
}
DI void put_flag(rsrc_t rs, u32 off, u64 tag) { st8(rs, off, 0, u32x2{(u32)tag, (u32)(tag >> 32)}); }
DI void signal4(rsrc_t rs, u32 lds0, u32 off, u64 tag, int lane) {
    WAITV(0);
    u32 old = 0;
    if (lane == 0) old = __hip_atomic_fetch_add((LAS u32*)(uintptr_t)(lds0 + L_CNT), 1u, __ATOMIC_RELAXED, __HIP_MEMORY_SCOPE_WORKGROUP);
    old = rfl(old);
    if ((old & 3) == 3 && lane == 0) put_flag(rs, off, tag);
}
template <int N> DI void dma_range(rsrc_t rs, u32 vl, u32 src, u32 dst, int dw) {
    src += dw * 1024; dst += dw * 1024;
#pragma unroll
    for (int i = 0; i < (N + 2) / 3; ++i) {
        if (3 * i + 2 < N || 3 * i + dw < N) dma16(rs, vl, src + i * 3072, dst + i * 3072);
    }
}

struct QkvW { static constexpr int off(int q) { return (((q >> 4) % 9) << 14) + (q & 15) * 1024; }
              static constexpr bool bar(int q) { return q == 0 || q == 48 || q == 96 || q == 144; } };
struct W1W { static constexpr int off(int q) { return q * 1024; }
             static constexpr bool bar(int q) { return q == 42 || q == 84; } };
struct WoW { static constexpr int off(int q) { return (q / 5) * 10240 + (q % 5) * 1024; }
             static constexpr bool bar(int q) { return q == 15; } };
struct W2W { static constexpr int off(int q) { return ((q / 15) % 5) * 30720 + ((q / 5) % 3) * 10240 + (q % 5) * 1024; }
             static constexpr bool bar(int q) { return q > 0 && q % 15 == 0; } };

template <int NT, int NSTEP, int NSWAP, class W>
DI void gemm_main(f32x4 (&acc)[NT][2], rsrc_t rs, u32 vx0, u32 vx1, u32 xso, u32 la0) {
    constexpr int PD = 2, WIN = 4, NQ = NT * NSTEP;
    u32x4 xr[NSTEP][2];
    u32x4 wf[NQ];
    const u32 la1 = la0 + 65536, la2 = la0 + 131072;
#pragma unroll
    for (int n = 0; n < NT; ++n) { acc[n][0] = f32x4{0.f, 0.f, 0.f, 0.f}; acc[n][1] = f32x4{0.f, 0.f, 0.f, 0.f}; }
    auto ldx = [&](auto I) { constexpr int s = I.v;
        gload<(s & 1) * 2048>(xr[s][0], rs, vx0, xso + (s >> 1) * 4096);
        gload<(s & 1) * 2048>(xr[s][1], rs, vx1, xso + (s >> 1) * 4096); };
    auto rdw = [&](auto I) { constexpr int q = I.v; constexpr int off = W::off(q);
        lread<(off & 0xffff)>(wf[q], (off >> 16) == 0 ? la0 : ((off >> 16) == 1 ? la1 : la2)); };
    sfor<0, PD>([&](auto I) { ldx(I); });
    if constexpr (W::bar(0)) BAR();
    sfor<0, WIN>([&](auto I) { rdw(I); });
    sfor<0, NSTEP>([&](auto I) { constexpr int s = I.v;
        if constexpr (s + PD < NSTEP) ldx(IC<s + PD>{});
        constexpr int last = (s + PD < NSTEP) ? s + PD : NSTEP - 1;
        asm volatile("s_waitcnt vmcnt(%0)" :: "n"(2 * (last - s)));
        SB();
        sfor<0, NT>([&](auto J) { constexpr int n = J.v; constexpr int q = s * NT + n, qn = q + WIN;
            if constexpr (qn < NQ && W::bar(qn)) BAR();
            asm volatile("s_waitcnt lgkmcnt(%0)" :: "n"(qn < NQ ? WIN - 1 : NQ - 1 - q)); SB();
            if constexpr (n < NSWAP) acc[n][0] = mfma(wf[q], xr[s][0], acc[n][0]); else acc[n][0] = mfma(xr[s][0], wf[q], acc[n][0]);
            SB();
            if constexpr (qn < NQ) { rdw(IC<qn>{}); SB(); }
            if constexpr (n < NSWAP) acc[n][1] = mfma(wf[q], xr[s][1], acc[n][1]); else acc[n][1] = mfma(xr[s][1], wf[q], acc[n][1]);
            SB();
        });
    });
#pragma unroll
    for (int n = 0; n < NT; ++n) asm volatile("" : "+v"(acc[n][0]), "+v"(acc[n][1]));
}

struct StatRegs { u32x4 v[8]; };
template <int NP> DI void stat_load(StatRegs& r, rsrc_t rs, u32 base, int lane) {
#pragma unroll
    for (int p = 0; p < NP; ++p) r.v[p] = ld16(rs, lane * 16, base + p * 1024);
}
template <int NP, bool TWICE> DI void stat_finish(const StatRegs& r, u32 lds0, int lane) {
    float s0 = 0.f, q0 = 0.f, s1 = 0.f, q1 = 0.f;
#pragma unroll
    for (int p = 0; p < NP; ++p) { s0 += f_of(r.v[p].x); q0 += f_of(r.v[p].y); s1 += f_of(r.v[p].z); q1 += f_of(r.v[p].w); }
    auto fin = [&](float s, float q, float& A, float& B) {
        const float m = s * (1.f / 640.f);
        const float v = fmaxf(q * (1.f / 640.f) - m * m, 0.f);
        float r1 = 1.f / sqrtf(v + 1e-5f);
        if (TWICE) r1 *= 1.f / sqrtf(v * r1 * r1 + 1e-5f);
        A = r1; B = -m * r1; };
    float A0, B0, A1, B1;
    fin(s0, q0, A0, B0); fin(s1, q1, A1, B1);
    lds_st<f32x2>(lds0 + L_SA + lane * 8, f32x2{A0, A1});
    lds_st<f32x2>(lds0 + L_SB + lane * 8, f32x2{B0, B1});
}

DI int qkv_col(int j, int n, int f) {
    const int a = 40 * j;
    if (n < 4) return (n >> 1) * 640 + a + (n & 1) * 20 + f;
    if (n == 4) return (f >> 3) * 640 + a + ((f >> 2) & 1) * 20 + 16 + (f & 3);
    if (n < 7) return 1280 + a + (n - 5) * 20 + f;
    return f < 8 ? 1280 + a + (f >> 2) * 20 + 16 + (f & 3) : -1;
}
DI void prologue(const u16* x, const u16* wqkv, const u16* wo, const u16* w1, const u16* w2, rsrc_t rs, u32 lds0, int c, int j, int wid,
                 int lane, int tid, u64 tag) {
    const int f = lane & 15, g = lane >> 4;
    const int tok = 128 * c + 8 * j + wid;
    const rsrc_t rx = __builtin_amdgcn_make_buffer_rsrc((void*)x, 0, 2048 * 1280, 0x00020000);
    const u32x4 xa = __builtin_amdgcn_raw_buffer_load_b128(rx, lane * 16, tok * 1280, 0);
    const u32x4 xb = __builtin_amdgcn_raw_buffer_load_b128(rx, lane < 16 ? 1024 + lane * 16 : 0x80000000u, tok * 1280, 0);
    u16 wv[4][8]; u32 dst[4], csd[4]; bool ok[4];
#pragma unroll
    for (int i = 0; i < 4; ++i) {
        const int e = wid + 8 * i;
        const u16* src = w2; u32 bytes = 1728 * 640 * 2; int ld = 640, rA = 0, rB = 0, col = -1; u32 d = 0, cs = 0; bool valid = e < 31;
        if (e < 10) {
            const int p = 10 * c + e, s = p >> 3, n = p & 7;
            src = wqkv; bytes = 640 * 1920 * 2; ld = 1920; rA = 32 * s + 8 * g; rB = rA + 4; col = qkv_col(j, n, f);
            d = WS_PQ + j * 163840 + p * 1024; cs = WS_CSQ + (j * 20 + s) * 512 + n * 64;
        } else if (e < 19) {
            const int p = c + 16 * (e - 10), s = p / 7, n = p % 7, tb = (27 * j) >> 2, cnt = (j & 3) ? 7 : 6;
            valid = p < 140;
            src = w1; bytes = 640 * 1728 * 2; ld = 1728; rA = 32 * s + 8 * g; rB = rA + 4; col = n < cnt ? 16 * (tb + n) + f : -1;
            d = WS_P1 + j * 143360 + p * 1024; cs = WS_CS1 + (j * 20 + s) * 512 + n * 64;
        } else {
            const int p = 2 * c + (j >> 3) + 32 * (e - 19), jj = j & 7;
            valid = valid && p < 370;
            if (p < 100) {
                const int it = p / 10, kh = (p / 5) & 1, n = p % 5, U = 4 * (kh * 10 + it) + g, jp = U / 5, u = U % 5;
                src = wo; bytes = 640 * 640 * 2;
                rA = 40 * jp + (u == 4 ? 16 : (u >> 1) * 20 + (u & 1) * 8); rB = u == 4 ? 40 * jp + 36 : rA + 4;
                col = 80 * jj + 16 * n + f; d = WS_PO + jj * 102400 + p * 1024;
            } else {
                const int p2 = p - 100, it = p2 / 10, kh = (p2 / 5) & 1, n = p2 % 5;
                rA = 32 * (kh * 27 + it) + 8 * g; rB = rA + 4; col = 80 * jj + 16 * n + f; d = WS_P2 + jj * 276480 + p2 * 1024;
            }
        }
        const rsrc_t rw = __builtin_amdgcn_make_buffer_rsrc((void*)src, 0, bytes, 0x00020000);
        const bool live = valid && col >= 0;
        const u32 vA = live ? (u32)(rA * ld + col) * 2 : 0x80000000u, vB = live ? (u32)(rB * ld + col) * 2 : 0x80000000u;
#pragma unroll
        for (int k = 0; k < 4; ++k) {
            wv[i][k] = __builtin_amdgcn_raw_buffer_load_b16(rw, vA, k * ld * 2, 0);
            wv[i][4 + k] = __builtin_amdgcn_raw_buffer_load_b16(rw, vB, k * ld * 2, 0);
        }
        dst[i] = d; csd[i] = cs; ok[i] = valid;
    }
    if (tid == 0) lds_st<u32>(lds0 + L_CNT, 0u);
    {
        float s = 0.f, q = 0.f;
#pragma unroll
        for (int k = 0; k < 4; ++k) {
            const float a0 = bflo(xa[k]), a1 = bfhi(xa[k]), b0 = bflo(xb[k]), b1 = bfhi(xb[k]);
            s += a0 + a1 + b0 + b1; q += a0 * a0 + a1 * a1 + b0 * b0 + b1 * b1;
        }
#pragma unroll
        for (int o = 1; o < 16; o <<= 1) { s += __shfl_xor(s, o); q += __shfl_xor(q, o); }
        s = rows_sum(s); q = rows_sum(q);
        if (lane == 0) st8(rs, 0, WS_ST + c * 8192 + (8 * j + wid) * 8, u32x2{u_of(s), u_of(q)});
        lds_st<u32x4>(lds0 + lane * 144 + wid * 16, xa);
        if (lane < 16) lds_st<u32x4>(lds0 + (64 + lane) * 144 + wid * 16, xb);
        LBAR();
        const u32 base = WS_X0 + (4 * c + (j >> 2)) * XT + (j & 3) * 128;
        { const int q0 = tid; st16(rs, (q0 >> 3) * 512 + (q0 & 7) * 16, base, lds_ld<u32x4>(lds0 + (q0 >> 3) * 144 + (q0 & 7) * 16)); }
        if (tid < 128) { const int q0 = tid + 512; st16(rs, (q0 >> 3) * 512 + (q0 & 7) * 16, base, lds_ld<u32x4>(lds0 + (q0 >> 3) * 144 + (q0 & 7) * 16)); }
    }
#pragma unroll
    for (int i = 0; i < 4; ++i) {
        if (ok[i]) {
            u32x4 v;
#pragma unroll
            for (int k = 0; k < 4; ++k) v[k] = (u32)wv[i][2 * k] | ((u32)wv[i][2 * k + 1] << 16);
            st16(rs, lane * 16, dst[i], v);
            if (csd[i]) {
                float s = 0.f;
#pragma unroll
                for (int k = 0; k < 4; ++k) s += bflo(v[k]) + bfhi(v[k]);
                s = rows_sum(s);
                if (lane < 16) st4(rs, lane * 4, csd[i], u_of(s));
            }
        }
    }
    WAITV(0);
    LBAR();
    if (tid == 0) put_flag(rs, WS_FROW + (c * 16 + j) * 8, tag);
    if (tid == 64) put_flag(rs, WS_FCOL + (j * 16 + c) * 8, tag);
}

template <int MODE>
DI void att_scores(u32 kaddr, const u32x4 (&qf)[2], const f32x4 (&cin)[2], f32x4 (&s)[2][2]) {
    const u32x4 k0 = lds_ld<u32x4>(kaddr), k1 = lds_ld<u32x4>(kaddr + 1024);
    s[0][0] = mfma(k0, qf[0], cin[0]);
    if (MODE != 1) s[1][0] = mfma(k1, qf[0], cin[0]);
    if (MODE != 2) s[0][1] = mfma(k0, qf[1], cin[1]);
    s[1][1] = mfma(k1, qf[1], cin[1]);
}
template <int MODE>
DI void att_max(u32 kaddr, const u32x4 (&qf)[2], float (&m)[2], int t, int g) {
    const f32x4 z[2] = {f32x4{0.f, 0.f, 0.f, 0.f}, f32x4{0.f, 0.f, 0.f, 0.f}};
    f32x4 s[2][2];
    att_scores<MODE>(kaddr, qf, z, s);
    constexpr float NEG = -3.0e38f;
    if (MODE != 0) {
#pragma unroll
        for (int r = 0; r < 4; ++r) {
            const bool keep = MODE == 1 ? (4 * g + r <= t) : (4 * g + r > t);
            s[0][0][r] = keep ? s[0][0][r] : NEG; s[1][1][r] = keep ? s[1][1][r] : NEG;
        }
        if (MODE == 1) s[1][0] = f32x4{NEG, NEG, NEG, NEG}; else s[0][1] = f32x4{NEG, NEG, NEG, NEG};
    }
#pragma unroll
    for (int q = 0; q < 2; ++q)
        m[q] = fmaxf(fmaxf(fmaxf(m[q], fmaxf(s[0][q][0], s[0][q][1])), fmaxf(s[0][q][2], s[0][q][3])),
                     fmaxf(fmaxf(s[1][q][0], s[1][q][1]), fmaxf(s[1][q][2], s[1][q][3])));
}
template <int MODE>
DI void att_pv(u32 gaddr, int hd, const u32x4 (&qf)[2], const f32x4 (&nm)[2], f32x4 (&o)[2], f32x4 (&orr)[2], int t, int g) {
    f32x4 s[2][2];
    const u32x4 v = lds_ld<u32x4>(gaddr + 4096 + hd * 1024), vr = lds_ld<u32x4>(gaddr + 6144);
    att_scores<MODE>(gaddr + hd * 2048, qf, nm, s);
    u32x4 p[2];
#pragma unroll
    for (int q = 0; q < 2; ++q) {
        float e[2][4];
#pragma unroll
        for (int ks = 0; ks < 2; ++ks) {
            const bool dead = (MODE == 1 && ks == 1 && q == 0) || (MODE == 2 && ks == 0 && q == 1);
#pragma unroll
            for (int r = 0; r < 4; ++r) {
                float ev = dead ? 0.f : __builtin_amdgcn_exp2f(s[ks][q][r]);
                if (MODE != 0 && ks == q) { const bool keep = MODE == 1 ? (4 * g + r <= t) : (4 * g + r > t); ev = keep ? ev : 0.f; }
                e[ks][r] = ev;
            }
        }
        p[q] = u32x4{pk(e[0][0], e[0][1]), pk(e[0][2], e[0][3]), pk(e[1][0], e[1][1]), pk(e[1][2], e[1][3])};
    }
#pragma unroll
    for (int q = 0; q < 2; ++q) { o[q] = mfma(v, p[q], o[q]); orr[q] = mfma(vr, p[q], orr[q]); }
}

DI void down_epilogue(f32x4 (&acc)[5][2], const u32x2 (&res)[5], bool bias, bool last, u32 dstx, u32 stbuf, u32 flag, rsrc_t rs, u32 lds0,
                      u16* out, int c, int j, int wid, int lane, u64 tag) {
    const int t = lane & 15, g = lane >> 4, jj = j & 7, hf = j >> 3, tt = wid & 1, kh = wid >> 1; const u32 vl = lane * 16;
#pragma unroll
    for (int n = 0; n < 5; ++n) lds_st<f32x4>(lds0 + L_EX + wid * 5120 + n * 1024 + vl, acc[n][1]);
    LBAR();
    f32x4 v[5];
    float s = 0.f, q = 0.f;
#pragma unroll
    for (int n = 0; n < 5; ++n) {
        v[n] = acc[n][0] + lds_ld<f32x4>(lds0 + L_EX + (wid ^ 2) * 5120 + n * 1024 + vl);
        v[n] += f32x4{bflo(res[n].x), bfhi(res[n].x), bflo(res[n].y), bfhi(res[n].y)};
        if (bias) v[n] += lds_ld<f32x4>(lds0 + L_B2 + (16 * n + 4 * g) * 4);
#pragma unroll
        for (int r = 0; r < 4; ++r) { s += v[n][r]; q += v[n][r] * v[n][r]; }
    }
    u32x2 p[5];
#pragma unroll
    for (int n = 0; n < 5; ++n) p[n] = pk4(v[n]);
    const u32x4 c01 = swap_pair(p[0], p[1]), c23 = swap_pair(p[2], p[3]), c44 = swap_pair(p[4], p[4]);
    const int T = 16 * kh + t, tile = 4 * c + 2 * hf + tt;
    if (!last) {
        const u32 base = dstx + tile * XT + 10 * jj * 512;
        const u32 vo = (2 * (g & 1) + (g >> 1)) * 512 + T * 16;
        st16(rs, vo, base, c01);
        st16(rs, vo, base + 2048, c23);
        if ((g & 1) == 0) st16(rs, (g >> 1) * 512 + T * 16, base + 4096, c44);
        s = rows_sum(s); q = rows_sum(q);
        if (g == 0) st8(rs, (64 * hf + 32 * tt + T) * 8, stbuf + c * 8192 + jj * 1024, u32x2{u_of(s), u_of(q)});
    } else {
        u16* orow = out + (size_t)(tile * 32 + T) * 640 + 80 * jj + 16 * (g & 1) + 8 * (g >> 1);
        *(u32x4*)(orow) = c01;
        *(u32x4*)(orow + 32) = c23;
        if ((g & 1) == 0) *(u32x4*)(orow + 64) = c44;
    }
    LBAR();
    if (!last) signal4(rs, lds0, flag, tag, lane);
}
DI void res_load(u32x2 (&res)[5], u32 src, rsrc_t rs, int c, int j, int wid, int lane) {
    const int t = lane & 15, g = lane >> 4, jj = j & 7, hf = j >> 3, tt = wid & 1, kh = wid >> 1;
    const u32 base = src + (4 * c + 2 * hf + tt) * XT + 10 * jj * 512;
#pragma unroll
    for (int n = 0; n < 5; ++n) res[n] = ld8(rs, (g >> 1) * 512 + (16 * kh + t) * 16 + (g & 1) * 8, base + n * 1024);
}

__global__ __launch_bounds__(512, 2) void fused_layers(const u16* __restrict__ x, const u16* __restrict__ wqkv, const u16* __restrict__ wo,
                                                       const u16* __restrict__ w1, const u16* __restrict__ b1, const u16* __restrict__ w2,
                                                       const u16* __restrict__ b2, u16* __restrict__ out, unsigned char* __restrict__ ws) {
    extern __shared__ __attribute__((aligned(16))) char lds[];
    const int tid = threadIdx.x, lane_o = tid & 63, wid_o = rfl(tid >> 6);
    const int b = blockIdx.x, c_o = 2 * (b & 7) + (b >> 7), j_o = (b >> 3) & 15;
    const u32 lds0 = (u32)(uintptr_t)lds;
    const rsrc_t rs = __builtin_amdgcn_make_buffer_rsrc((void*)ws, 0, WS_END, 0x00020000);
    u64 tag;
    {
        const u32x2 e = ld8(rs, 0, WS_E);
        tag = (((u64)rfl(e.y) << 32) | rfl(e.x)) + 1;
    }
    int dead = 0;
    prologue(x, wqkv, wo, w1, w2, rs, lds0, c_o, j_o, wid_o, lane_o, tid, tag);

#pragma unroll 1
    for (int l = 0; l < 2; ++l) {
        if (STOP_AFTER < 10 * l + 1) break;
        { PHASE_VARS();
        if (wid_o < 4) {
            f32x4 acc[8][2];
            BAR();
            const u32 vx = g * 512 + t * 16;
            gemm_main<8, 20, 5, QkvW>(acc, rs, vx, vx + 256, xin + (4 * c + wid) * XT, lds0 + vl);
            BAR();
            constexpr float QS = 0.22360679774997896f * 1.4426950408889634f;
            float sA[2], sB[2]; f32x4 A4[2], B4[2];
#pragma unroll
            for (int u = 0; u < 2; ++u) {
                sA[u] = lds_ld<float>(lds0 + L_SA + (32 * wid + 16 * u + t) * 4); sB[u] = lds_ld<float>(lds0 + L_SB + (32 * wid + 16 * u + t) * 4);
                A4[u] = lds_ld<f32x4>(lds0 + L_SA + (32 * wid + 16 * u + 4 * g) * 4); B4[u] = lds_ld<f32x4>(lds0 + L_SB + (32 * wid + 16 * u + 4 * g) * 4);
            }
            u32x2 pq[8][2];
            sfor<0, 5>([&](auto N) { constexpr int n = N.v;
                const f32x4 cs = lds_ld<f32x4>(lds0 + L_CSQ + (16 * n + 4 * g) * 4);
                const float sc = n < 2 ? QS : (n == 4 ? (g < 2 ? QS : 1.f) : 1.f);
#pragma unroll
                for (int u = 0; u < 2; ++u) {
                    const float a = sA[u] * sc, bb = sB[u] * sc;
                    f32x4 y;
#pragma unroll
                    for (int r = 0; r < 4; ++r) y[r] = a * acc[n][u][r] + bb * cs[r];
                    pq[n][u] = pk4(y);
                }
            });
            sfor<5, 8>([&](auto N) { constexpr int n = N.v;
                const float cs = lds_ld<float>(lds0 + L_CSQ + (16 * n + t) * 4);
#pragma unroll
                for (int u = 0; u < 2; ++u) {
                    f32x4 y;
#pragma unroll
                    for (int r = 0; r < 4; ++r) y[r] = A4[u][r] * acc[n][u][r] + B4[u][r] * cs;
                    pq[n][u] = pk4(y);
                    if (n == 7) pq[n][u] = t == 8 ? u32x2{0x3f803f80u, 0x3f803f80u} : pq[n][u];
                }
            });
            const u32 kvl = lds0 + L_KV + (8 + wid) * KVG + vl, ql = lds0 + L_Q + wid * 4096 + vl;
            const u32 kvg = WS_KV + (l * 256 + c * 16 + j) * 4 * KVG + wid * KVG;
#pragma unroll
            for (int u = 0; u < 2; ++u) {
                auto kx = __builtin_amdgcn_permlane32_swap(pq[4][u].x, pq[4][u].x, false, false);
                auto ky = __builtin_amdgcn_permlane32_swap(pq[4][u].y, pq[4][u].y, false, false);
#pragma unroll
                for (int hd = 0; hd < 2; ++hd) {
                    const bool own = g == hd;
                    const u32x4 qfr = {pq[hd][u].x, pq[hd][u].y, own ? pq[4][u].x : 0u, own ? pq[4][u].y : 0u};
                    const u32x4 kfr = {pq[2 + hd][u].x, pq[2 + hd][u].y, own ? (u32)kx[1] : 0u, own ? (u32)ky[1] : 0u};
                    lds_st<u32x4>(ql + (hd * 2 + u) * 1024, qfr);
                    lds_st<u32x4>(kvl + (hd * 2 + u) * 1024, kfr);
                    st16(rs, vl, kvg + (hd * 2 + u) * 1024, kfr);
                }
            }
#pragma unroll
            for (int n = 0; n < 3; ++n) {
                const u32x4 vfr = {pq[5 + n][0].x, pq[5 + n][0].y, pq[5 + n][1].x, pq[5 + n][1].y};
                lds_st<u32x4>(kvl + 4096 + n * 1024, vfr);
                st16(rs, vl, kvg + 4096 + n * 1024, vfr);
            }
        } else if (wid_o == 4) {
            if (l == 0) {
                const u32 fo = lane < 16 ? WS_FROW + (c * 16 + lane) * 8
                             : (lane < 32 ? WS_FCOL + (j * 16 + lane - 16) * 8 : WS_FCOL + ((j ^ 8) * 16 + (lane & 15)) * 8);
                poll(rs, fo, tag, dead);
            } else {
                poll(rs, WS_PH + PH_X2 * 2048 + c * 128 + t * 8, tag, dead);
            }
            BAR();
            BAR();
            StatRegs sr;
            if (l == 0) {
                stat_load<1>(sr, rs, WS_ST + c * 8192, lane);
                u32x4 cq[10], c1[10];
                const int qd = lane & 31, hs = lane >> 5;
#pragma unroll
                for (int i = 0; i < 10; ++i) {
                    cq[i] = ld16(rs, (hs * 10 + i) * 512 + qd * 16, WS_CSQ + j * 10240);
                    c1[i] = ld16(rs, qd < 28 ? (hs * 10 + i) * 512 + qd * 16 : 0x80000000u, WS_CS1 + j * 10240);
                }
                const int tb = (27 * j) >> 2, cnt = (j & 3) ? 7 : 6;
                const u16 bb0 = lane < 16 * cnt ? b1[16 * tb + lane] : (u16)0;
                const u16 bb1 = lane + 64 < 16 * cnt ? b1[16 * tb + lane + 64] : (u16)0;
                const u16 c0 = b2[80 * jj + lane];
                const u16 c1b = lane < 16 ? b2[80 * jj + 64 + lane] : (u16)0;
                BAR();
                stat_finish<1, false>(sr, lds0, lane);
                f32x4 sq = {0.f, 0.f, 0.f, 0.f}, s1 = {0.f, 0.f, 0.f, 0.f};
#pragma unroll
                for (int i = 0; i < 10; ++i) { sq += __builtin_bit_cast(f32x4, cq[i]); s1 += __builtin_bit_cast(f32x4, c1[i]); }
#pragma unroll
                for (int r = 0; r < 4; ++r) { sq[r] += __shfl_xor(sq[r], 32); s1[r] += __shfl_xor(s1[r], 32); }
                if (lane < 32) { lds_st<f32x4>(lds0 + L_CSQ + qd * 16, sq); lds_st<f32x4>(lds0 + L_CS1 + qd * 16, s1); }
                lds_st<float>(lds0 + L_B1 + lane * 4, bflo(bb0)); lds_st<float>(lds0 + L_B1 + 256 + lane * 4, bflo(bb1));
                lds_st<float>(lds0 + L_B2 + lane * 4, bflo(c0)); lds_st<float>(lds0 + L_B2 + 256 + lane * 4, bflo(c1b));
            } else {
                stat_load<8>(sr, rs, WS_ST + 2 * 131072 + c * 8192, lane);
                BAR();
                stat_finish<8, false>(sr, lds0, lane);
            }
            BAR();
            BAR();
            LBAR();
        } else {
            const u32 src = WS_PQ + j * 163840;
            BAR();
            if (l == 0) dma_range<144>(rs, vl, src, lds0, dw);
            WAITV(32); BAR();
            WAITV(16); BAR();
            dma_range<16>(rs, vl, src + 144 * 1024, lds0, dw);
            WAITV(5); BAR();
            WAITV(0); BAR();
            BAR();
        }
        }
        if (STOP_AFTER < 10 * l + 2) { WAITV(0); break; }
        { PHASE_VARS();
            LBAR();
            const int hd = wid >> 2, w = hd ? 3 - (wid & 3) : wid;
            u32x4 qf[2];
            qf[0] = lds_ld<u32x4>(lds0 + L_Q + w * 4096 + hd * 2048 + vl);
            qf[1] = lds_ld<u32x4>(lds0 + L_Q + w * 4096 + hd * 2048 + 1024 + vl);
            const u32 kv0 = lds0 + L_KV + vl;
            float m[2] = {-3.0e38f, -3.0e38f};
#pragma unroll 1
            for (int q = 8; q < 8 + w; ++q) att_max<0>(kv0 + q * KVG + hd * 2048, qf, m, t, g);
            att_max<1>(kv0 + (8 + w) * KVG + hd * 2048, qf, m, t, g);
            if (wid < 4) signal4(rs, lds0, WS_KVF + (l * 16 + j) * 128 + c * 8, tag, lane);
            m[0] = rows_max(m[0]); m[1] = rows_max(m[1]);
            f32x4 nm[2] = {f32x4{-m[0], -m[0], -m[0], -m[0]}, f32x4{-m[1], -m[1], -m[1], -m[1]}};
            f32x4 o[2] = {f32x4{0.f, 0.f, 0.f, 0.f}, f32x4{0.f, 0.f, 0.f, 0.f}}, orr[2] = {f32x4{0.f, 0.f, 0.f, 0.f}, f32x4{0.f, 0.f, 0.f, 0.f}};
#pragma unroll 1
            for (int q = 8; q < 8 + w; ++q) att_pv<0>(kv0 + q * KVG, hd, qf, nm, o, orr, t, g);
            att_pv<1>(kv0 + (8 + w) * KVG, hd, qf, nm, o, orr, t, g);
            if (c > 0) {
                const int np = c >= 2 ? 2 : 1;
                if (wid == 4) poll(rs, WS_KVF + (l * 16 + j) * 128 + (c - 1 - (lane & (np - 1))) * 8, tag, dead);
                BAR();
                {
                    u32x4 kv[7];
                    const int pn = wid >> 2;
                    if (pn == 1 || np == 2) {
                        const u32 src = WS_KV + (l * 256 + (c - 2 + pn) * 16 + j) * 4 * KVG + (wid & 3) * KVG;
#pragma unroll
                        for (int i = 0; i < 7; ++i) kv[i] = ld16(rs, vl, src + i * 1024);
#pragma unroll
                        for (int i = 0; i < 7; ++i) lds_st<u32x4>(kv0 + wid * KVG + i * 1024, kv[i]);
                    }
                }
                LBAR();
                float m2[2] = {m[0], m[1]};
                int q0 = 4;
                if (np == 2) { att_max<2>(kv0 + w * KVG + hd * 2048, qf, m2, t, g); q0 = w + 1; }
#pragma unroll 1
                for (int q = q0; q < 8; ++q) att_max<0>(kv0 + q * KVG + hd * 2048, qf, m2, t, g);
#pragma unroll
                for (int q = 0; q < 2; ++q) {
                    m2[q] = rows_max(m2[q]);
                    const float sc = __builtin_amdgcn_exp2f(m[q] - m2[q]);
                    o[q] *= sc; orr[q] *= sc;
                    nm[q] = f32x4{-m2[q], -m2[q], -m2[q], -m2[q]};
                }
                if (np == 2) att_pv<2>(kv0 + w * KVG, hd, qf, nm, o, orr, t, g);
#pragma unroll 1
                for (int q = q0; q < 8; ++q) att_pv<0>(kv0 + q * KVG, hd, qf, nm, o, orr, t, g);
            }
            const u32 ob = WS_O + (l * 64 + 4 * c + w) * XT;
            u32x2 po[2], pr[2];
#pragma unroll
            for (int q = 0; q < 2; ++q) {
                auto a = __builtin_amdgcn_permlane32_swap(u_of(orr[q][0]), u_of(orr[q][0]), false, false);
                auto bb = __builtin_amdgcn_permlane16_swap((u32)a[1], (u32)a[1], false, false);
                const float inv = 1.f / f_of(bb[0]);
                po[q] = pk4(o[q] * inv); pr[q] = pk4(orr[q] * inv);
            }
            st16(rs, vl, ob + (5 * j + 2 * hd) * 512, swap_pair(po[0], po[1]));
            if (g == hd) {
                st8(rs, t * 16 + hd * 8, ob + (5 * j + 4) * 512, pr[0]);
                st8(rs, t * 16 + hd * 8 + 256, ob + (5 * j + 4) * 512, pr[1]);
            }
            WAITV(0);
            LBAR();
            if (tid == 0) put_flag(rs, ph + PH_O * 2048 + j * 8, tag);
        }
        if (STOP_AFTER < 10 * l + 3) break;
        { PHASE_VARS();
        if (wid_o < 4) {
            const int tt = wid & 1, kh = wid >> 1;
            u32x2 res[5];
            res_load(res, xin, rs, c, j, wid, lane);
            f32x4 acc[5][2];
            BAR();
            const u32 vx = g * 512 + t * 16;
            gemm_main<5, 10, 5, WoW>(acc, rs, vx + kh * 256, vx + (kh ^ 1) * 256, WS_O + (l * 64 + 4 * c + 2 * hf + tt) * XT + kh * 10 * 2048,
                                     lds0 + vl + kh * 5120);
            down_epilogue(acc, res, false, false, WS_X1 + l * 64 * XT, WS_ST + (1 + 2 * l) * 131072, ph + PH_X1 * 2048 + j * 8, rs, lds0, out, c, j, wid, lane, tag);
        } else if (wid_o == 4) {
            poll(rs, ph + PH_O * 2048 + t * 8, tag, dead);
            BAR(); BAR(); BAR(); BAR();
        } else {
            dma_range<100>(rs, vl, WS_PO + jj * 102400, lds0, dw);
            WAITV(23); BAR();
            WAITV(0); BAR();
            BAR(); BAR();
            dma_range<140>(rs, vl, WS_P1 + j * 143360, lds0, dw);
        }
        }
        if (STOP_AFTER < 10 * l + 4) { WAITV(0); break; }
        { PHASE_VARS();
        if (wid_o < 4) {
            f32x4 acc[7][2];
            BAR();
            const u32 vx = g * 512 + t * 16;
            gemm_main<7, 20, 7, W1W>(acc, rs, vx, vx + 256, WS_X1 + (l * 64 + 4 * c + wid) * XT, lds0 + vl);
            BAR();
            float sA[2], sB[2];
#pragma unroll
            for (int u = 0; u < 2; ++u) {
                sA[u] = lds_ld<float>(lds0 + L_SA + (32 * wid + 16 * u + t) * 4); sB[u] = lds_ld<float>(lds0 + L_SB + (32 * wid + 16 * u + t) * 4);
            }
            const int tb = (27 * j) >> 2, cnt = (j & 3) ? 7 : 6;
            const u32 hb = WS_H + (l * 64 + 4 * c + wid) * HT + tb * 1024;
            constexpr float C0 = -2.f * 0.7978845608028654f * 1.4426950408889634f, C1 = C0 * 0.044715f;
            sfor<0, 7>([&](auto N) { constexpr int n = N.v;
                if (n < 6 || cnt == 7) {
                    const f32x4 cs = lds_ld<f32x4>(lds0 + L_CS1 + (16 * n + 4 * g) * 4), bs = lds_ld<f32x4>(lds0 + L_B1 + (16 * n + 4 * g) * 4);
                    u32x2 p[2];
#pragma unroll
                    for (int u = 0; u < 2; ++u) {
                        f32x4 h;
#pragma unroll
                        for (int r = 0; r < 4; ++r) {
                            const float y = sA[u] * acc[n][u][r] + (sB[u] * cs[r] + bs[r]);
                            const float e = __builtin_amdgcn_exp2f(y * (C0 + C1 * y * y));
                            h[r] = y * __builtin_amdgcn_rcpf(1.f + e);
                        }
                        p[u] = pk4(h);
                    }
                    st16(rs, vl, hb + n * 1024, swap_pair(p[0], p[1]));
                }
            });
            signal4(rs, lds0, ph + PH_H * 2048 + j * 8, tag, lane);
        } else if (wid_o == 4) {
            poll(rs, ph + PH_X1 * 2048 + t * 8, tag, dead);
            BAR();
            StatRegs sr;
            stat_load<8>(sr, rs, WS_ST + (1 + 2 * l) * 131072 + c * 8192, lane);
            BAR();
            stat_finish<8, true>(sr, lds0, lane);
            BAR();
            LBAR();
        } else {
            WAITV(32); BAR();
            WAITV(18); BAR();
            WAITV(0); BAR();
            BAR();
            dma_range<150>(rs, vl, WS_P2 + jj * 276480, lds0, dw);
        }
        }
        if (STOP_AFTER < 10 * l + 5) { WAITV(0); break; }
        { PHASE_VARS();
        if (wid_o < 4) {
            const int tt = wid & 1, kh = wid >> 1;
            u32x2 res[5];
            res_load(res, WS_X1 + l * 64 * XT, rs, c, j, wid, lane);
            f32x4 acc[5][2];
            BAR();
            const u32 vx = g * 512 + t * 16;
            gemm_main<5, 27, 5, W2W>(acc, rs, vx + kh * 256, vx + (kh ^ 1) * 256, WS_H + (l * 64 + 4 * c + 2 * hf + tt) * HT + kh * 27 * 2048,
                                     lds0 + vl + kh * 5120);
            down_epilogue(acc, res, true, l == 1, WS_X0 + 64 * XT, WS_ST + 2 * 131072, WS_PH + PH_X2 * 2048 + c * 128 + j * 8, rs, lds0, out, c, j, wid,
                          lane, tag);
        } else if (wid_o == 4) {
            poll(rs, ph + PH_H * 2048 + t * 8, tag, dead);
#pragma unroll
            for (int i = 0; i < 11; ++i) BAR();
        } else {
            const u32 src = WS_P2 + jj * 276480;
            WAITV(40); BAR();
            WAITV(30); BAR();
#pragma unroll
            for (int m = 2; m < 6; ++m) {
                WAITV(20); BAR();
                dma_range<30>(rs, vl, src + (m + 3) * 30720, lds0 + (m - 2) * 30720, dw);
            }
            WAITV(20); BAR();
            WAITV(10); BAR();
            WAITV(0); BAR();
            BAR(); BAR();
            if (l == 0) dma_range<144>(rs, vl, WS_PQ + j * 163840, lds0, dw);
        }
        }
    }
    WAITV(0);
    if (b == 0 && tid == 256) put_flag(rs, WS_E, tag);
}

extern "C" void kernel_launch(void* const* d_in, const int* in_sizes, int n_in, void* d_out, int out_size, void* d_ws, size_t ws_size,
                              hipStream_t stream) {
    (void)in_sizes; (void)n_in; (void)out_size; (void)ws_size;
    (void)hipFuncSetAttribute((const void*)fused_layers, hipFuncAttributeMaxDynamicSharedMemorySize, LDS_BYTES);
    fused_layers<<<256, 512, LDS_BYTES, stream>>>((const u16*)d_in[0], (const u16*)d_in[1], (const u16*)d_in[2], (const u16*)d_in[4],
                                                  (const u16*)d_in[5], (const u16*)d_in[6], (const u16*)d_in[7], (u16*)d_out,
                                                  (unsigned char*)d_ws);
}
```

```cpp
#include <hip/hip_runtime.h>
#include <hip/hip_bf16.h>
#include <stdint.h>

#define LAS __attribute__((address_space(3)))
#define DI __device__ __forceinline__
typedef unsigned u32;
typedef unsigned short u16;
typedef unsigned long long u64;
typedef unsigned u32x2 __attribute__((ext_vector_type(2)));
typedef unsigned u32x4 __attribute__((ext_vector_type(4)));
typedef float f32x2 __attribute__((ext_vector_type(2)));
typedef float f32x4 __attribute__((ext_vector_type(4)));
typedef __bf16 bf16x2 __attribute__((ext_vector_type(2)));
typedef __bf16 bf16x8 __attribute__((ext_vector_type(8)));
typedef __amdgpu_buffer_rsrc_t rsrc_t;

#define PD_Q 4
#define PD_O 10
#define PD_1 4
#define PD_2 6
#define MAPPING 1
#define PRE_Q 96
#define PRE_1 96
#define STOP_AFTER 99

constexpr u32 XT = 80 * 512;
constexpr u32 HT = 216 * 512;
constexpr u32 KVG = 7168;
constexpr u32 WS_E = 0;
constexpr u32 WS_FROW = 4096;
constexpr u32 WS_FCOL = WS_FROW + 2048;
constexpr u32 WS_KVF = WS_FCOL + 2048;
constexpr u32 WS_PH = WS_KVF + 4096;
constexpr u32 WS_ST = 65536;
constexpr u32 WS_CSQ = WS_ST + 4 * 131072;
constexpr u32 WS_CS1 = WS_CSQ + 16 * 20 * 512;
constexpr u32 WS_PQ = WS_CS1 + 16 * 20 * 512;
constexpr u32 WS_P1 = WS_PQ + 16 * 163840;
constexpr u32 WS_PO = WS_P1 + 16 * 143360;
constexpr u32 WS_P2 = WS_PO + 8 * 102400;
constexpr u32 WS_X0 = WS_P2 + 8 * 276480;
constexpr u32 WS_X1 = WS_X0 + 2 * 64 * XT;
constexpr u32 WS_O = WS_X1 + 2 * 64 * XT;
constexpr u32 WS_H = WS_O + 2 * 64 * XT;
constexpr u32 WS_KV = WS_H + 2 * 64 * HT;
constexpr u32 WS_DBG = WS_KV + 2 * 256 * 4 * KVG;
constexpr u32 WS_END = WS_DBG + 4096 + 262144;
enum { PH_O = 0, PH_X1 = 1, PH_H = 2, PH_X2 = 3 };

constexpr u32 L_KV = 0;
constexpr u32 L_Q = 12 * KVG;
constexpr u32 L_EXO = 51200, L_EX2 = 122880;
constexpr u32 L_MISC = 153600;
constexpr u32 L_SA = L_MISC, L_SB = L_MISC + 512, L_CSQ = L_MISC + 1024, L_CS1 = L_MISC + 1536, L_B1 = L_MISC + 2048, L_B2 = L_MISC + 2560,
              L_CNT = L_MISC + 3072;
constexpr u32 LDS_BYTES = L_MISC + 6144;

#define TS(role, idx) do { } while (0)
template <int I> struct IC { static constexpr int v = I; };
template <int B, int E, class F> DI void sfor(F&& f) { if constexpr (B < E) { f(IC<B>{}); sfor<B + 1, E>(f); } }
DI u32 rfl(u32 x) { return __builtin_amdgcn_readfirstlane(x); }
DI f32x4 mfma(u32x4 a, u32x4 b, f32x4 c) {
    return __builtin_amdgcn_mfma_f32_16x16x32_bf16(__builtin_bit_cast(bf16x8, a), __builtin_bit_cast(bf16x8, b), c, 0, 0, 0);
}
DI u32 pk(float a, float b) { f32x2 v = {a, b}; return __builtin_bit_cast(u32, __builtin_convertvector(v, bf16x2)); }
DI float bflo(u32 w) { return __builtin_bit_cast(float, w << 16); }
DI float bfhi(u32 w) { return __builtin_bit_cast(float, w & 0xffff0000u); }
DI float f_of(u32 w) { return __builtin_bit_cast(float, w); }
DI u32 u_of(float f) { return __builtin_bit_cast(u32, f); }
#define SB() __builtin_amdgcn_sched_barrier(0)
#define BAR() asm volatile("s_barrier" ::: "memory")
#define LBAR() asm volatile("s_waitcnt lgkmcnt(0)\n\ts_barrier" ::: "memory")
#define PHASE_VARS() \
    int lane = lane_o, c = c_o, j = j_o, wid = wid_o; \
    asm volatile("" : "+v"(lane), "+s"(c), "+s"(j), "+s"(wid)); \
    const int t = lane & 15, g = lane >> 4, jj = j & 7, hf = j >> 3, dw = wid - 5; const u32 vl = lane * 16; \
    const u32 xin = WS_X0 + l * 64 * XT, ph = WS_PH + l * 4 * 2048 + c * 128; \
    (void)t; (void)g; (void)jj; (void)hf; (void)dw; (void)vl; (void)xin; (void)ph;
#define WAITV(n) asm volatile("s_waitcnt vmcnt(%0)" :: "n"(n) : "memory")
DI void dma16(rsrc_t rs, u32 voff, u32 soff, u32 ldsaddr) {
    asm volatile("s_mov_b32 m0, %0\n\ts_nop 0\n\tbuffer_load_dwordx4 %1, %2, %3 offen sc1 lds" :: "s"(ldsaddr), "v"(voff), "s"(rs), "s"(soff) : "memory");
}
template <int OFF> DI void gload(u32x4& d, rsrc_t rs, u32 voff, u32 soff) {
    asm volatile("buffer_load_dwordx4 %0, %1, %2, %3 offen offset:%4 sc1" : "=v"(d) : "v"(voff), "s"(rs), "s"(soff), "n"(OFF));
}
template <int OFF> DI void lread(u32x4& d, u32 addr) { asm volatile("ds_read_b128 %0, %1 offset:%2" : "=v"(d) : "v"(addr), "n"(OFF)); }
DI void st16(rsrc_t rs, u32 voff, u32 soff, u32x4 v) { __builtin_amdgcn_raw_buffer_store_b128(v, rs, voff, soff, 16); }
DI void st8(rsrc_t rs, u32 voff, u32 soff, u32x2 v) { __builtin_amdgcn_raw_buffer_store_b64(v, rs, voff, soff, 16); }
DI void st4(rsrc_t rs, u32 voff, u32 soff, u32 v) { __builtin_amdgcn_raw_buffer_store_b32(v, rs, voff, soff, 16); }
DI u32x4 ld16(rsrc_t rs, u32 voff, u32 soff) { return __builtin_amdgcn_raw_buffer_load_b128(rs, voff, soff, 16); }
DI u32x2 ld8(rsrc_t rs, u32 voff, u32 soff) { return __builtin_amdgcn_raw_buffer_load_b64(rs, voff, soff, 16); }
template <class T> DI T lds_ld(u32 a) { return *(const LAS T*)(uintptr_t)a; }
template <class T> DI void lds_st(u32 a, T v) { *(LAS T*)(uintptr_t)a = v; }
DI float rows_sum(float v) {
    auto a = __builtin_amdgcn_permlane32_swap(u_of(v), u_of(v), false, false); v = f_of(a[0]) + f_of(a[1]);
    auto b = __builtin_amdgcn_permlane16_swap(u_of(v), u_of(v), false, false); return f_of(b[0]) + f_of(b[1]);
}
DI float rows_max(float v) {
    auto a = __builtin_amdgcn_permlane32_swap(u_of(v), u_of(v), false, false); v = fmaxf(f_of(a[0]), f_of(a[1]));
    auto b = __builtin_amdgcn_permlane16_swap(u_of(v), u_of(v), false, false); return fmaxf(f_of(b[0]), f_of(b[1]));
}
DI u32x4 swap_pair(u32x2 p, u32x2 q) {
    auto a = __builtin_amdgcn_permlane16_swap(p.x, q.x, false, false);
    auto b = __builtin_amdgcn_permlane16_swap(p.y, q.y, false, false);
    return u32x4{a[0], b[0], a[1], b[1]};
}
DI u32x2 pk4(f32x4 v) { return u32x2{pk(v[0], v[1]), pk(v[2], v[3])}; }

DI void poll(rsrc_t rs, u32 voff, u64 tag, int& dead) {
    if (dead) return;
    for (int it = 0; it < (1 << 19); ++it) {
        u32x2 v;
        asm volatile("buffer_load_dwordx2 %0, %1, %2, 0 offen sc1\n\ts_waitcnt vmcnt(0)" : "=v"(v) : "v"(voff), "s"(rs) : "memory");
        if (__builtin_amdgcn_ballot_w64(v.x != (u32)tag || v.y != (u32)(tag >> 32)) == 0) return;
        __builtin_amdgcn_s_sleep(1);
    }
    dead = 1;
}
DI void put_flag(rsrc_t rs, u32 off, u64 tag) { st8(rs, off, 0, u32x2{(u32)tag, (u32)(tag >> 32)}); }
DI void signal4(rsrc_t rs, u32 lds0, u32 off, u64 tag, int lane) {
    WAITV(0);
    u32 old = 0;
    if (lane == 0) old = __hip_atomic_fetch_add((LAS u32*)(uintptr_t)(lds0 + L_CNT), 1u, __ATOMIC_RELAXED, __HIP_MEMORY_SCOPE_WORKGROUP);
    old = rfl(old);
    if ((old & 3) == 3 && lane == 0) put_flag(rs, off, tag);
}
template <int N> DI void dma_range(rsrc_t rs, u32 vl, u32 src, u32 dst, int dw) {
    src += dw * 1024; dst += dw * 1024;
#pragma unroll
    for (int i = 0; i < (N + 2) / 3; ++i) {
        if (3 * i + 2 < N || 3 * i + dw < N) dma16(rs, vl, src + i * 3072, dst + i * 3072);
    }
}

struct QkvW { static constexpr int off(int q) { return (((q >> 4) % 9) << 14) + (q & 15) * 1024; }
              static constexpr bool bar(int q) { return q == 0 || q == 48 || q == PRE_Q || q == 144; } };
struct W1W { static constexpr int off(int q) { return q * 1024; }
             static constexpr bool bar(int q) { return q == 42 || q == PRE_1; } };
struct WoW { static constexpr int off(int q) { return (q < 25 ? 102400 + (q / 5) * 10240 : (q / 5 - 5) * 10240) + (q % 5) * 1024; }
             static constexpr bool bar(int) { return false; } };
struct W2W { static constexpr int off(int q) { return ((q / 15) % 5) * 30720 + ((q / 5) % 3) * 10240 + (q % 5) * 1024; }
             static constexpr bool bar(int q) { return q > 0 && q % 15 == 0; } };

template <int NT, int NSTEP, int NSWAP, class W, int PD>
DI void gemm_main(f32x4 (&acc)[NT][2], rsrc_t rs, u32 vx0, u32 vx1, u32 xso, u32 la0) {
    constexpr int WIN = 4, NQ = NT * NSTEP;
    u32x4 xr[NSTEP][2];
    u32x4 wf[NQ];
    const u32 la1 = la0 + 65536, la2 = la0 + 131072;
#pragma unroll
    for (int n = 0; n < NT; ++n) { acc[n][0] = f32x4{0.f, 0.f, 0.f, 0.f}; acc[n][1] = f32x4{0.f, 0.f, 0.f, 0.f}; }
    auto ldx = [&](auto I) { constexpr int s = I.v;
        gload<(s & 1) * 2048>(xr[s][0], rs, vx0, xso + (s >> 1) * 4096);
        gload<(s & 1) * 2048>(xr[s][1], rs, vx1, xso + (s >> 1) * 4096); };
    auto rdw = [&](auto I) { constexpr int q = I.v; constexpr int off = W::off(q);
        lread<(off & 0xffff)>(wf[q], (off >> 16) == 0 ? la0 : ((off >> 16) == 1 ? la1 : la2)); };
    sfor<0, PD>([&](auto I) { ldx(I); });
    if constexpr (W::bar(0)) BAR();
    sfor<0, WIN>([&](auto I) { rdw(I); });
    sfor<0, NSTEP>([&](auto I) { constexpr int s = I.v;
        if constexpr (s + PD < NSTEP) ldx(IC<s + PD>{});
        constexpr int last = (s + PD < NSTEP) ? s + PD : NSTEP - 1;
        asm volatile("s_waitcnt vmcnt(%0)" :: "n"(2 * (last - s)));
        SB();
        sfor<0, NT>([&](auto J) { constexpr int n = J.v; constexpr int q = s * NT + n, qn = q + WIN;
            if constexpr (qn < NQ && W::bar(qn)) BAR();
            asm volatile("s_waitcnt lgkmcnt(%0)" :: "n"(qn < NQ ? WIN - 1 : NQ - 1 - q)); SB();
            if constexpr (n < NSWAP) acc[n][0] = mfma(wf[q], xr[s][0], acc[n][0]); else acc[n][0] = mfma(xr[s][0], wf[q], acc[n][0]);
            SB();
            if constexpr (qn < NQ) { rdw(IC<qn>{}); SB(); }
            if constexpr (n < NSWAP) acc[n][1] = mfma(wf[q], xr[s][1], acc[n][1]); else acc[n][1] = mfma(xr[s][1], wf[q], acc[n][1]);
            SB();
        });
    });
#pragma unroll
    for (int n = 0; n < NT; ++n) asm volatile("" : "+v"(acc[n][0]), "+v"(acc[n][1]));
}

struct StatRegs { u32x4 v[8]; };
template <int NP> DI void stat_load(StatRegs& r, rsrc_t rs, u32 base, int lane) {
#pragma unroll
    for (int p = 0; p < NP; ++p) r.v[p] = ld16(rs, lane * 16, base + p * 1024);
}
template <int NP, bool TWICE> DI void stat_finish(const StatRegs& r, u32 lds0, int lane) {
    float s0 = 0.f, q0 = 0.f, s1 = 0.f, q1 = 0.f;
#pragma unroll
    for (int p = 0; p < NP; ++p) { s0 += f_of(r.v[p].x); q0 += f_of(r.v[p].y); s1 += f_of(r.v[p].z); q1 += f_of(r.v[p].w); }
    auto fin = [&](float s, float q, float& A, float& B) {
        const float m = s * (1.f / 640.f);
        const float v = fmaxf(q * (1.f / 640.f) - m * m, 0.f);
        float r1 = 1.f / sqrtf(v + 1e-5f);
        if (TWICE) r1 *= 1.f / sqrtf(v * r1 * r1 + 1e-5f);
        A = r1; B = -m * r1; };
    float A0, B0, A1, B1;
    fin(s0, q0, A0, B0); fin(s1, q1, A1, B1);
    lds_st<f32x2>(lds0 + L_SA + lane * 8, f32x2{A0, A1});
    lds_st<f32x2>(lds0 + L_SB + lane * 8, f32x2{B0, B1});
}

DI int qkv_col(int j, int n, int f) {
    const int a = 40 * j;
    if (n < 4) return (n >> 1) * 640 + a + (n & 1) * 20 + f;
    if (n == 4) return (f >> 3) * 640 + a + ((f >> 2) & 1) * 20 + 16 + (f & 3);
    if (n < 7) return 1280 + a + (n - 5) * 20 + f;
    return f < 8 ? 1280 + a + (f >> 2) * 20 + 16 + (f & 3) : -1;
}
DI void prologue(const u16* x, const u16* wqkv, const u16* wo, const u16* w1, const u16* w2, rsrc_t rs, u32 lds0, int c, int j, int wid,
                 int lane, int tid, u64 tag) {
    const int f = lane & 15, g = lane >> 4;
    const int tok = 128 * c + 8 * j + wid;
    const rsrc_t rx = __builtin_amdgcn_make_buffer_rsrc((void*)x, 0, 2048 * 1280, 0x00020000);
    const u32x4 xa = __builtin_amdgcn_raw_buffer_load_b128(rx, lane * 16, tok * 1280, 0);
    const u32x4 xb = __builtin_amdgcn_raw_buffer_load_b128(rx, lane < 16 ? 1024 + lane * 16 : 0x80000000u, tok * 1280, 0);
    u16 wv[4][8]; u32 dst[4], csd[4]; bool ok[4];
#pragma unroll
    for (int i = 0; i < 4; ++i) {
        const int e = wid + 8 * i;
        const u16* src = w2; u32 bytes = 1728 * 640 * 2; int ld = 640, rA = 0, rB = 0, col = -1; u32 d = 0, cs = 0; bool valid = e < 31;
        if (e < 10) {
            const int p = 10 * c + e, s = p >> 3, n = p & 7;
            src = wqkv; bytes = 640 * 1920 * 2; ld = 1920; rA = 32 * s + 8 * g; rB = rA + 4; col = qkv_col(j, n, f);
            d = WS_PQ + j * 163840 + p * 1024; cs = WS_CSQ + (j * 20 + s) * 512 + n * 64;
        } else if (e < 19) {
            const int p = c + 16 * (e - 10), s = p / 7, n = p % 7, tb = (27 * j) >> 2, cnt = (j & 3) ? 7 : 6;
            valid = p < 140;
            src = w1; bytes = 640 * 1728 * 2; ld = 1728; rA = 32 * s + 8 * g; rB = rA + 4; col = n < cnt ? 16 * (tb + n) + f : -1;
            d = WS_P1 + j * 143360 + p * 1024; cs = WS_CS1 + (j * 20 + s) * 512 + n * 64;
        } else {
            const int p = 2 * c + (j >> 3) + 32 * (e - 19), jj = j & 7;
            valid = valid && p < 370;
            if (p < 100) {
                const int it = p / 10, kh = (p / 5) & 1, n = p % 5, U = 4 * (kh * 10 + it) + g, jp = U / 5, u = U % 5;
                src = wo; bytes = 640 * 640 * 2;
                rA = 40 * jp + (u == 4 ? 16 : (u >> 1) * 20 + (u & 1) * 8); rB = u == 4 ? 40 * jp + 36 : rA + 4;
                col = 80 * jj + 16 * n + f; d = WS_PO + jj * 102400 + p * 1024;
            } else {
                const int p2 = p - 100, it = p2 / 10, kh = (p2 / 5) & 1, n = p2 % 5;
                rA = 32 * (kh * 27 + it) + 8 * g; rB = rA + 4; col = 80 * jj + 16 * n + f; d = WS_P2 + jj * 276480 + p2 * 1024;
            }
        }
        const rsrc_t rw = __builtin_amdgcn_make_buffer_rsrc((void*)src, 0, bytes, 0x00020000);
        const bool live = valid && col >= 0;
        const u32 vA = live ? (u32)(rA * ld + col) * 2 : 0x80000000u, vB = live ? (u32)(rB * ld + col) * 2 : 0x80000000u;
#pragma unroll
        for (int k = 0; k < 4; ++k) {
            wv[i][k] = __builtin_amdgcn_raw_buffer_load_b16(rw, vA, k * ld * 2, 0);
            wv[i][4 + k] = __builtin_amdgcn_raw_buffer_load_b16(rw, vB, k * ld * 2, 0);
        }
        dst[i] = d; csd[i] = cs; ok[i] = valid;
    }
    if (tid == 0) lds_st<u32>(lds0 + L_CNT, 0u);
    {
        float s = 0.f, q = 0.f;
#pragma unroll
        for (int k = 0; k < 4; ++k) {
            const float a0 = bflo(xa[k]), a1 = bfhi(xa[k]), b0 = bflo(xb[k]), b1 = bfhi(xb[k]);
            s += a0 + a1 + b0 + b1; q += a0 * a0 + a1 * a1 + b0 * b0 + b1 * b1;
        }
#pragma unroll
        for (int o = 1; o < 16; o <<= 1) { s += __shfl_xor(s, o); q += __shfl_xor(q, o); }
        s = rows_sum(s); q = rows_sum(q);
        if (lane == 0) st8(rs, 0, WS_ST + c * 8192 + (8 * j + wid) * 8, u32x2{u_of(s), u_of(q)});
        lds_st<u32x4>(lds0 + lane * 144 + wid * 16, xa);
        if (lane < 16) lds_st<u32x4>(lds0 + (64 + lane) * 144 + wid * 16, xb);
        LBAR();
        const u32 base = WS_X0 + (4 * c + (j >> 2)) * XT + (j & 3) * 128;
        { const int q0 = tid; st16(rs, (q0 >> 3) * 512 + (q0 & 7) * 16, base, lds_ld<u32x4>(lds0 + (q0 >> 3) * 144 + (q0 & 7) * 16)); }
        if (tid < 128) { const int q0 = tid + 512; st16(rs, (q0 >> 3) * 512 + (q0 & 7) * 16, base, lds_ld<u32x4>(lds0 + (q0 >> 3) * 144 + (q0 & 7) * 16)); }
    }
#pragma unroll
    for (int i = 0; i < 4; ++i) {
        if (ok[i]) {
            u32x4 v;
#pragma unroll
            for (int k = 0; k < 4; ++k) v[k] = (u32)wv[i][2 * k] | ((u32)wv[i][2 * k + 1] << 16);
            st16(rs, lane * 16, dst[i], v);
            if (csd[i]) {
                float s = 0.f;
#pragma unroll
                for (int k = 0; k < 4; ++k) s += bflo(v[k]) + bfhi(v[k]);
                s = rows_sum(s);
                if (lane < 16) st4(rs, lane * 4, csd[i], u_of(s));
            }
        }
    }
    WAITV(0);
    LBAR();
    if (tid == 0) put_flag(rs, WS_FROW + (c * 16 + j) * 8, tag);
    if (tid == 64) put_flag(rs, WS_FCOL + (j * 16 + c) * 8, tag);
}

template <int MODE>
DI void att_scores(u32x4 k0, u32x4 k1, const u32x4 (&qf)[2], const f32x4 (&cin)[2], f32x4 (&s)[2][2]) {
    s[0][0] = mfma(k0, qf[0], cin[0]);
    if (MODE != 1) s[1][0] = mfma(k1, qf[0], cin[0]);
    if (MODE != 2) s[0][1] = mfma(k0, qf[1], cin[1]);
    s[1][1] = mfma(k1, qf[1], cin[1]);
}
template <int MODE, int N>
DI void att_max(u32 kaddr, const u32x4 (&qf)[2], float (&m)[2], int t, int g) {
    const f32x4 z[2] = {f32x4{0.f, 0.f, 0.f, 0.f}, f32x4{0.f, 0.f, 0.f, 0.f}};
    u32x4 k[N][2];
#pragma unroll
    for (int i = 0; i < N; ++i) { k[i][0] = lds_ld<u32x4>(kaddr + i * KVG); k[i][1] = lds_ld<u32x4>(kaddr + i * KVG + 1024); }
    f32x4 s[N][2][2];
#pragma unroll
    for (int i = 0; i < N; ++i) att_scores<MODE>(k[i][0], k[i][1], qf, z, s[i]);
    constexpr float NEG = -3.0e38f;
#pragma unroll
    for (int i = 0; i < N; ++i) {
        if (MODE != 0) {
#pragma unroll
            for (int r = 0; r < 4; ++r) {
                const bool keep = MODE == 1 ? (4 * g + r <= t) : (4 * g + r > t);
                s[i][0][0][r] = keep ? s[i][0][0][r] : NEG; s[i][1][1][r] = keep ? s[i][1][1][r] : NEG;
            }
        }
#pragma unroll
        for (int q = 0; q < 2; ++q) {
#pragma unroll
            for (int ks = 0; ks < 2; ++ks) {
                if ((MODE == 1 && ks == 1 && q == 0) || (MODE == 2 && ks == 0 && q == 1)) continue;
                m[q] = fmaxf(fmaxf(m[q], s[i][ks][q][0]), s[i][ks][q][1]);
                m[q] = fmaxf(fmaxf(m[q], s[i][ks][q][2]), s[i][ks][q][3]);
            }
        }
    }
}
template <int MODE, int N>
DI void att_pv(u32 gaddr, int hd, const u32x4 (&qf)[2], const f32x4 (&nm)[2], f32x4 (&o)[2], f32x4 (&orr)[2], int t, int g) {
    u32x4 k[N][2], v[N], vr[N];
#pragma unroll
    for (int i = 0; i < N; ++i) { k[i][0] = lds_ld<u32x4>(gaddr + i * KVG + hd * 2048); k[i][1] = lds_ld<u32x4>(gaddr + i * KVG + hd * 2048 + 1024); }
#pragma unroll
    for (int i = 0; i < N; ++i) { v[i] = lds_ld<u32x4>(gaddr + i * KVG + 4096 + hd * 1024); vr[i] = lds_ld<u32x4>(gaddr + i * KVG + 6144); }
    f32x4 s[N][2][2];
#pragma unroll
    for (int i = 0; i < N; ++i) att_scores<MODE>(k[i][0], k[i][1], qf, nm, s[i]);
#pragma unroll
    for (int i = 0; i < N; ++i) {
#pragma unroll
        for (int q = 0; q < 2; ++q) {
            float e[2][4];
#pragma unroll
            for (int ks = 0; ks < 2; ++ks) {
                const bool dead = (MODE == 1 && ks == 1 && q == 0) || (MODE == 2 && ks == 0 && q == 1);
#pragma unroll
                for (int r = 0; r < 4; ++r) {
                    float ev = dead ? 0.f : __builtin_amdgcn_exp2f(s[i][ks][q][r]);
                    if (MODE != 0 && ks == q) { const bool keep = MODE == 1 ? (4 * g + r <= t) : (4 * g + r > t); ev = keep ? ev : 0.f; }
                    e[ks][r] = ev;
                }
            }
            const u32x4 p = {pk(e[0][0], e[0][1]), pk(e[0][2], e[0][3]), pk(e[1][0], e[1][1]), pk(e[1][2], e[1][3])};
            o[q] = mfma(v[i], p, o[q]); orr[q] = mfma(vr[i], p, orr[q]);
        }
    }
}
DI void att_max_range(u32 kv0, int hd, int qa, int qb, const u32x4 (&qf)[2], float (&m)[2], int t, int g) {
    int q = qa;
#pragma unroll 1
    for (; q + 1 < qb; q += 2) att_max<0, 2>(kv0 + q * KVG + hd * 2048, qf, m, t, g);
    if (q < qb) att_max<0, 1>(kv0 + q * KVG + hd * 2048, qf, m, t, g);
}
DI void att_pv_range(u32 kv0, int hd, int qa, int qb, const u32x4 (&qf)[2], const f32x4 (&nm)[2], f32x4 (&o)[2], f32x4 (&orr)[2], int t, int g) {
    int q = qa;
#pragma unroll 1
    for (; q + 1 < qb; q += 2) att_pv<0, 2>(kv0 + q * KVG, hd, qf, nm, o, orr, t, g);
    if (q < qb) att_pv<0, 1>(kv0 + q * KVG, hd, qf, nm, o, orr, t, g);
}

DI void down_epilogue(f32x4 (&acc)[5][2], const u32x2 (&res)[5], u32 lex, bool bias, bool last, u32 dstx, u32 stbuf, u32 flag, rsrc_t rs, u32 lds0,
                      u16* out, int c, int j, int wid, int lane, u64 tag, int tsb) {
    const int wid_o = wid, lane_o = lane; (void)wid_o; (void)lane_o; (void)tsb;
    const int t = lane & 15, g = lane >> 4, jj = j & 7, hf = j >> 3, tt = wid & 1, kh = wid >> 1; const u32 vl = lane * 16;
#pragma unroll
    for (int n = 0; n < 5; ++n) lds_st<f32x4>(lds0 + lex + wid * 5120 + n * 1024 + vl, acc[n][1]);
    TS(0, tsb);
    LBAR();
    TS(0, tsb + 1);
    f32x4 v[5];
    float s = 0.f, q = 0.f;
#pragma unroll
    for (int n = 0; n < 5; ++n) {
        v[n] = acc[n][0] + lds_ld<f32x4>(lds0 + lex + (wid ^ 2) * 5120 + n * 1024 + vl);
        v[n] += f32x4{bflo(res[n].x), bfhi(res[n].x), bflo(res[n].y), bfhi(res[n].y)};
        if (bias) v[n] += lds_ld<f32x4>(lds0 + L_B2 + (16 * n + 4 * g) * 4);
#pragma unroll
        for (int r = 0; r < 4; ++r) { s += v[n][r]; q += v[n][r] * v[n][r]; }
    }
    u32x2 p[5];
#pragma unroll
    for (int n = 0; n < 5; ++n) p[n] = pk4(v[n]);
    const u32x4 c01 = swap_pair(p[0], p[1]), c23 = swap_pair(p[2], p[3]), c44 = swap_pair(p[4], p[4]);
    const int T = 16 * kh + t, tile = 4 * c + 2 * hf + tt;
    if (!last) {
        const u32 base = dstx + tile * XT + 10 * jj * 512;
        const u32 vo = (2 * (g & 1) + (g >> 1)) * 512 + T * 16;
        st16(rs, vo, base, c01);
        st16(rs, vo, base + 2048, c23);
        if ((g & 1) == 0) st16(rs, (g >> 1) * 512 + T * 16, base + 4096, c44);
        s = rows_sum(s); q = rows_sum(q);
        if (g == 0) st8(rs, (64 * hf + 32 * tt + T) * 8, stbuf + c * 8192 + jj * 1024, u32x2{u_of(s), u_of(q)});
    } else {
        u16* orow = out + (size_t)(tile * 32 + T) * 640 + 80 * jj + 16 * (g & 1) + 8 * (g >> 1);
        *(u32x4*)(orow) = c01;
        *(u32x4*)(orow + 32) = c23;
        if ((g & 1) == 0) *(u32x4*)(orow + 64) = c44;
    }
    LBAR();
    TS(0, tsb + 2);
    if (!last) signal4(rs, lds0, flag, tag, lane);
    TS(0, tsb + 3);
}
DI void res_load(u32x2 (&res)[5], u32 src, rsrc_t rs, int c, int j, int wid, int lane) {
    const int t = lane & 15, g = lane >> 4, jj = j & 7, hf = j >> 3, tt = wid & 1, kh = wid >> 1;
    const u32 base = src + (4 * c + 2 * hf + tt) * XT + 10 * jj * 512;
#pragma unroll
    for (int n = 0; n < 5; ++n) res[n] = ld8(rs, (g >> 1) * 512 + (16 * kh + t) * 16 + (g & 1) * 8, base + n * 1024);
}

__global__ __launch_bounds__(512, 2) void fused_layers(const u16* __restrict__ x, const u16* __restrict__ wqkv, const u16* __restrict__ wo,
                                                       const u16* __restrict__ w1, const u16* __restrict__ b1, const u16* __restrict__ w2,
                                                       const u16* __restrict__ b2, u16* __restrict__ out, unsigned char* __restrict__ ws) {
    extern __shared__ __attribute__((aligned(16))) char lds[];
    const int tid = threadIdx.x, lane_o = tid & 63, wid_o = rfl(tid >> 6);
    const int b = blockIdx.x;
    const int c_o = 8 * (b & 1) + (b >> 5), j_o = 4 * ((b >> 1) & 3) + ((b >> 3) & 3);
    const u32 lds0 = (u32)(uintptr_t)lds;
    const rsrc_t rs = __builtin_amdgcn_make_buffer_rsrc((void*)ws, 0, WS_END, 0x00020000);
    u64 tag;
    {
        const u32x2 e = ld8(rs, 0, WS_E);
        tag = (((u64)rfl(e.y) << 32) | rfl(e.x)) + 1;
    }
    int dead = 0;
    TS(0, 0);
    prologue(x, wqkv, wo, w1, w2, rs, lds0, c_o, j_o, wid_o, lane_o, tid, tag);
    TS(0, 1);

#pragma unroll 1
    for (int l = 0; l < 2; ++l) {
        if (STOP_AFTER < 10 * l + 1) break;
        const int tb_ = 2 + 28 * l; (void)tb_;
        { PHASE_VARS();
        if (wid_o < 4) {
            f32x4 acc[8][2];
            BAR();
            TS(0, tb_ + 0);
            const u32 vx = g * 512 + t * 16;
            gemm_main<8, 20, 5, QkvW, PD_Q>(acc, rs, vx, vx + 256, xin + (4 * c + wid) * XT, lds0 + vl);
            TS(0, tb_ + 1);
            BAR();
            TS(0, tb_ + 2);
            constexpr float QS = 0.22360679774997896f * 1.4426950408889634f;
            float sA[2], sB[2]; f32x4 A4[2], B4[2];
#pragma unroll
            for (int u = 0; u < 2; ++u) {
                sA[u] = lds_ld<float>(lds0 + L_SA + (32 * wid + 16 * u + t) * 4); sB[u] = lds_ld<float>(lds0 + L_SB + (32 * wid + 16 * u + t) * 4);
                A4[u] = lds_ld<f32x4>(lds0 + L_SA + (32 * wid + 16 * u + 4 * g) * 4); B4[u] = lds_ld<f32x4>(lds0 + L_SB + (32 * wid + 16 * u + 4 * g) * 4);
            }
            u32x2 pq[8][2];
            sfor<0, 5>([&](auto N) { constexpr int n = N.v;
                const f32x4 cs = lds_ld<f32x4>(lds0 + L_CSQ + (16 * n + 4 * g) * 4);
                const float sc = n < 2 ? QS : (n == 4 ? (g < 2 ? QS : 1.f) : 1.f);
#pragma unroll
                for (int u = 0; u < 2; ++u) {
                    const float a = sA[u] * sc, bb = sB[u] * sc;
                    f32x4 y;
#pragma unroll
                    for (int r = 0; r < 4; ++r) y[r] = a * acc[n][u][r] + bb * cs[r];
                    pq[n][u] = pk4(y);
                }
            });
            sfor<5, 8>([&](auto N) { constexpr int n = N.v;
                const float cs = lds_ld<float>(lds0 + L_CSQ + (16 * n + t) * 4);
#pragma unroll
                for (int u = 0; u < 2; ++u) {
                    f32x4 y;
#pragma unroll
                    for (int r = 0; r < 4; ++r) y[r] = A4[u][r] * acc[n][u][r] + B4[u][r] * cs;
                    pq[n][u] = pk4(y);
                    if (n == 7) pq[n][u] = t == 8 ? u32x2{0x3f803f80u, 0x3f803f80u} : pq[n][u];
                }
            });
            const u32 kvl = lds0 + L_KV + (8 + wid) * KVG + vl, ql = lds0 + L_Q + wid * 4096 + vl;
            const u32 kvg = WS_KV + (l * 256 + c * 16 + j) * 4 * KVG + wid * KVG;
#pragma unroll
            for (int u = 0; u < 2; ++u) {
                auto kx = __builtin_amdgcn_permlane32_swap(pq[4][u].x, pq[4][u].x, false, false);
                auto ky = __builtin_amdgcn_permlane32_swap(pq[4][u].y, pq[4][u].y, false, false);
#pragma unroll
                for (int hd = 0; hd < 2; ++hd) {
                    const bool own = g == hd;
                    const u32x4 qfr = {pq[hd][u].x, pq[hd][u].y, own ? pq[4][u].x : 0u, own ? pq[4][u].y : 0u};
                    const u32x4 kfr = {pq[2 + hd][u].x, pq[2 + hd][u].y, own ? (u32)kx[1] : 0u, own ? (u32)ky[1] : 0u};
                    lds_st<u32x4>(ql + (hd * 2 + u) * 1024, qfr);
                    lds_st<u32x4>(kvl + (hd * 2 + u) * 1024, kfr);
                    st16(rs, vl, kvg + (hd * 2 + u) * 1024, kfr);
                }
            }
#pragma unroll
            for (int n = 0; n < 3; ++n) {
                const u32x4 vfr = {pq[5 + n][0].x, pq[5 + n][0].y, pq[5 + n][1].x, pq[5 + n][1].y};
                lds_st<u32x4>(kvl + 4096 + n * 1024, vfr);
                st16(rs, vl, kvg + 4096 + n * 1024, vfr);
            }
        } else if (wid_o == 4) {
            TS(1, tb_ + 0);
            if (l == 0) {
                const u32 fo = lane < 16 ? WS_FROW + (c * 16 + lane) * 8
                             : (lane < 32 ? WS_FCOL + (j * 16 + lane - 16) * 8 : WS_FCOL + ((j ^ 8) * 16 + (lane & 15)) * 8);
                poll(rs, fo, tag, dead);
            } else {
                poll(rs, WS_PH + PH_X2 * 2048 + c * 128 + t * 8, tag, dead);
            }
            TS(1, tb_ + 1);
            BAR();
            BAR();
            StatRegs sr;
            if (l == 0) {
                stat_load<1>(sr, rs, WS_ST + c * 8192, lane);
                u32x4 cq[10], c1[10];
                const int qd = lane & 31, hs = lane >> 5;
#pragma unroll
                for (int i = 0; i < 10; ++i) {
                    cq[i] = ld16(rs, (hs * 10 + i) * 512 + qd * 16, WS_CSQ + j * 10240);
                    c1[i] = ld16(rs, qd < 28 ? (hs * 10 + i) * 512 + qd * 16 : 0x80000000u, WS_CS1 + j * 10240);
                }
                const int tb = (27 * j) >> 2, cnt = (j & 3) ? 7 : 6;
                const u16 bb0 = lane < 16 * cnt ? b1[16 * tb + lane] : (u16)0;
                const u16 bb1 = lane + 64 < 16 * cnt ? b1[16 * tb + lane + 64] : (u16)0;
                const u16 c0 = b2[80 * jj + lane];
                const u16 c1b = lane < 16 ? b2[80 * jj + 64 + lane] : (u16)0;
                BAR();
                stat_finish<1, false>(sr, lds0, lane);
                f32x4 sq = {0.f, 0.f, 0.f, 0.f}, s1 = {0.f, 0.f, 0.f, 0.f};
#pragma unroll
                for (int i = 0; i < 10; ++i) { sq += __builtin_bit_cast(f32x4, cq[i]); s1 += __builtin_bit_cast(f32x4, c1[i]); }
#pragma unroll
                for (int r = 0; r < 4; ++r) { sq[r] += __shfl_xor(sq[r], 32); s1[r] += __shfl_xor(s1[r], 32); }
                if (lane < 32) { lds_st<f32x4>(lds0 + L_CSQ + qd * 16, sq); lds_st<f32x4>(lds0 + L_CS1 + qd * 16, s1); }
                lds_st<float>(lds0 + L_B1 + lane * 4, bflo(bb0)); lds_st<float>(lds0 + L_B1 + 256 + lane * 4, bflo(bb1));
                lds_st<float>(lds0 + L_B2 + lane * 4, bflo(c0)); lds_st<float>(lds0 + L_B2 + 256 + lane * 4, bflo(c1b));
            } else {
                stat_load<8>(sr, rs, WS_ST + 2 * 131072 + c * 8192, lane);
                BAR();
                stat_finish<8, false>(sr, lds0, lane);
            }
            BAR();
            BAR();
            LBAR();
        } else {
            const u32 src = WS_PQ + j * 163840;
            BAR();
            if (l == 0) dma_range<PRE_Q>(rs, vl, src, lds0, dw);
            TS(2, tb_ + 0);
            WAITV((PRE_Q - 48) / 3); TS(2, tb_ + 1); BAR();
            dma_range<144 - PRE_Q>(rs, vl, src + PRE_Q * 1024, lds0 + PRE_Q * 1024, dw);
            WAITV((144 - PRE_Q) / 3); TS(2, tb_ + 2); BAR();
            dma_range<16>(rs, vl, src + 144 * 1024, lds0, dw);
            WAITV(5); TS(2, tb_ + 3); BAR();
            WAITV(0); TS(2, tb_ + 4); BAR();
            BAR();
            dma_range<50>(rs, vl, WS_PO + jj * 102400, lds0 + 102400, dw);
        }
        }
        if (STOP_AFTER < 10 * l + 2) { WAITV(0); break; }
        { PHASE_VARS();
            TS(0, tb_ + 3);
            LBAR();
            TS(0, tb_ + 4);
            const int hd = wid >> 2, w = hd ? 3 - (wid & 3) : wid;
            u32x4 qf[2];
            qf[0] = lds_ld<u32x4>(lds0 + L_Q + w * 4096 + hd * 2048 + vl);
            qf[1] = lds_ld<u32x4>(lds0 + L_Q + w * 4096 + hd * 2048 + 1024 + vl);
            const u32 kv0 = lds0 + L_KV + vl;
            float m[2] = {-3.0e38f, -3.0e38f};
            att_max_range(kv0, hd, 8, 8 + w, qf, m, t, g);
            att_max<1, 1>(kv0 + (8 + w) * KVG + hd * 2048, qf, m, t, g);
            if (wid < 4) signal4(rs, lds0, WS_KVF + (l * 16 + j) * 128 + c * 8, tag, lane);
            TS(0, tb_ + 5);
            const int pc = c - 2 + (wid >> 2);
            u32x4 kv[7];
            if (pc >= 0) {
                TS(1, tb_ + 2);
                poll(rs, WS_KVF + (l * 16 + j) * 128 + pc * 8, tag, dead);
                TS(1, tb_ + 3);
                const u32 src = WS_KV + (l * 256 + pc * 16 + j) * 4 * KVG + (wid & 3) * KVG;
#pragma unroll
                for (int i = 0; i < 7; ++i) kv[i] = ld16(rs, vl, src + i * 1024);
                asm volatile("" ::: "memory");
            }
            m[0] = rows_max(m[0]); m[1] = rows_max(m[1]);
            f32x4 nm[2] = {f32x4{-m[0], -m[0], -m[0], -m[0]}, f32x4{-m[1], -m[1], -m[1], -m[1]}};
            f32x4 o[2] = {f32x4{0.f, 0.f, 0.f, 0.f}, f32x4{0.f, 0.f, 0.f, 0.f}}, orr[2] = {f32x4{0.f, 0.f, 0.f, 0.f}, f32x4{0.f, 0.f, 0.f, 0.f}};
            att_pv_range(kv0, hd, 8, 8 + w, qf, nm, o, orr, t, g);
            att_pv<1, 1>(kv0 + (8 + w) * KVG, hd, qf, nm, o, orr, t, g);
            TS(0, tb_ + 6);
            if (c > 0) {
                const int np = c >= 2 ? 2 : 1;
                if (pc >= 0) {
#pragma unroll
                    for (int i = 0; i < 7; ++i) lds_st<u32x4>(kv0 + wid * KVG + i * 1024, kv[i]);
                }
                TS(0, tb_ + 7);
                LBAR();
                TS(0, tb_ + 8);
                float m2[2] = {m[0], m[1]};
                int q0 = 4;
                if (np == 2) { att_max<2, 1>(kv0 + w * KVG + hd * 2048, qf, m2, t, g); q0 = w + 1; }
                att_max_range(kv0, hd, q0, 8, qf, m2, t, g);
#pragma unroll
                for (int q = 0; q < 2; ++q) {
                    m2[q] = rows_max(m2[q]);
                    const float sc = __builtin_amdgcn_exp2f(m[q] - m2[q]);
                    o[q] *= sc; orr[q] *= sc;
                    nm[q] = f32x4{-m2[q], -m2[q], -m2[q], -m2[q]};
                }
                if (np == 2) att_pv<2, 1>(kv0 + w * KVG, hd, qf, nm, o, orr, t, g);
                att_pv_range(kv0, hd, q0, 8, qf, nm, o, orr, t, g);
            }
            TS(0, tb_ + 9);
            const u32 ob = WS_O + (l * 64 + 4 * c + w) * XT;
            u32x2 po[2], pr[2];
#pragma unroll
            for (int q = 0; q < 2; ++q) {
                auto a = __builtin_amdgcn_permlane32_swap(u_of(orr[q][0]), u_of(orr[q][0]), false, false);
                auto bb = __builtin_amdgcn_permlane16_swap((u32)a[1], (u32)a[1], false, false);
                const float inv = 1.f / f_of(bb[0]);
                po[q] = pk4(o[q] * inv); pr[q] = pk4(orr[q] * inv);
            }
            st16(rs, vl, ob + (5 * j + 2 * hd) * 512, swap_pair(po[0], po[1]));
            if (g == hd) {
                st8(rs, t * 16 + hd * 8, ob + (5 * j + 4) * 512, pr[0]);
                st8(rs, t * 16 + hd * 8 + 256, ob + (5 * j + 4) * 512, pr[1]);
            }
            WAITV(0);
            LBAR();
            TS(0, tb_ + 10);
            if (tid == 0) put_flag(rs, ph + PH_O * 2048 + j * 8, tag);
        }
        if (STOP_AFTER < 10 * l + 3) break;
        { PHASE_VARS();
        if (wid_o < 4) {
            const int tt = wid & 1, kh = wid >> 1;
            u32x2 res[5];
            res_load(res, xin, rs, c, j, wid, lane);
            f32x4 acc[5][2];
            BAR();
            TS(0, tb_ + 11);
            const u32 vx = g * 512 + t * 16;
            gemm_main<5, 10, 5, WoW, PD_O>(acc, rs, vx + kh * 256, vx + (kh ^ 1) * 256, WS_O + (l * 64 + 4 * c + 2 * hf + tt) * XT + kh * 10 * 2048,
                                     lds0 + vl + kh * 5120);
            down_epilogue(acc, res, L_EXO, false, false, WS_X1 + l * 64 * XT, WS_ST + (1 + 2 * l) * 131072, ph + PH_X1 * 2048 + j * 8, rs, lds0, out, c, j, wid, lane, tag, tb_ + 12);
        } else if (wid_o == 4) {
            TS(1, tb_ + 4);
            poll(rs, ph + PH_O * 2048 + t * 8, tag, dead);
            TS(1, tb_ + 5);
            BAR(); BAR(); BAR();
        } else {
            dma_range<50>(rs, vl, WS_PO + jj * 102400 + 51200, lds0, dw);
            TS(2, tb_ + 5);
            WAITV(0); TS(2, tb_ + 6); TS(2, tb_ + 7); BAR();
            BAR(); BAR();
            dma_range<PRE_1>(rs, vl, WS_P1 + j * 143360, lds0, dw);
            TS(2, tb_ + 8);
        }
        }
        if (STOP_AFTER < 10 * l + 4) { WAITV(0); break; }
        { PHASE_VARS();
        if (wid_o < 4) {
            f32x4 acc[7][2];
            BAR();
            TS(0, tb_ + 16);
            const u32 vx = g * 512 + t * 16;
            gemm_main<7, 20, 7, W1W, PD_1>(acc, rs, vx, vx + 256, WS_X1 + (l * 64 + 4 * c + wid) * XT, lds0 + vl);
            TS(0, tb_ + 17);
            BAR();
            TS(0, tb_ + 18);
            float sA[2], sB[2];
#pragma unroll
            for (int u = 0; u < 2; ++u) {
                sA[u] = lds_ld<float>(lds0 + L_SA + (32 * wid + 16 * u + t) * 4); sB[u] = lds_ld<float>(lds0 + L_SB + (32 * wid + 16 * u + t) * 4);
            }
            const int tb = (27 * j) >> 2, cnt = (j & 3) ? 7 : 6;
            const u32 hb = WS_H + (l * 64 + 4 * c + wid) * HT + tb * 1024;
            constexpr float C0 = -2.f * 0.7978845608028654f * 1.4426950408889634f, C1 = C0 * 0.044715f;
            sfor<0, 7>([&](auto N) { constexpr int n = N.v;
                if (n < 6 || cnt == 7) {
                    const f32x4 cs = lds_ld<f32x4>(lds0 + L_CS1 + (16 * n + 4 * g) * 4), bs = lds_ld<f32x4>(lds0 + L_B1 + (16 * n + 4 * g) * 4);
                    u32x2 p[2];
#pragma unroll
                    for (int u = 0; u < 2; ++u) {
                        f32x4 h;
#pragma unroll
                        for (int r = 0; r < 4; ++r) {
                            const float y = sA[u] * acc[n][u][r] + (sB[u] * cs[r] + bs[r]);
                            const float e = __builtin_amdgcn_exp2f(y * (C0 + C1 * y * y));
                            h[r] = y * __builtin_amdgcn_rcpf(1.f + e);
                        }
                        p[u] = pk4(h);
                    }
                    st16(rs, vl, hb + n * 1024, swap_pair(p[0], p[1]));
                }
            });
            TS(0, tb_ + 19);
            signal4(rs, lds0, ph + PH_H * 2048 + j * 8, tag, lane);
            TS(0, tb_ + 20);
        } else if (wid_o == 4) {
            TS(1, tb_ + 6);
            poll(rs, ph + PH_X1 * 2048 + t * 8, tag, dead);
            TS(1, tb_ + 7);
            BAR();
            StatRegs sr;
            stat_load<8>(sr, rs, WS_ST + (1 + 2 * l) * 131072 + c * 8192, lane);
            BAR();
            stat_finish<8, true>(sr, lds0, lane);
            BAR();
            LBAR();
        } else {
            const u32 src = WS_P1 + j * 143360;
            WAITV((PRE_1 - 42) / 3); TS(2, tb_ + 9); BAR();
            dma_range<140 - PRE_1>(rs, vl, src + PRE_1 * 1024, lds0 + PRE_1 * 1024, dw);
            WAITV((140 - PRE_1) / 3); TS(2, tb_ + 10); BAR();
            WAITV(0); TS(2, tb_ + 11); BAR();
            BAR();
            dma_range<150>(rs, vl, WS_P2 + jj * 276480, lds0, dw);
            TS(2, tb_ + 12);
        }
        }
        if (STOP_AFTER < 10 * l + 5) { WAITV(0); break; }
        { PHASE_VARS();
        if (wid_o < 4) {
            const int tt = wid & 1, kh = wid >> 1;
            u32x2 res[5];
            res_load(res, WS_X1 + l * 64 * XT, rs, c, j, wid, lane);
            f32x4 acc[5][2];
            BAR();
            TS(0, tb_ + 21);
            const u32 vx = g * 512 + t * 16;
            gemm_main<5, 27, 5, W2W, PD_2>(acc, rs, vx + kh * 256, vx + (kh ^ 1) * 256, WS_H + (l * 64 + 4 * c + 2 * hf + tt) * HT + kh * 27 * 2048,
                                     lds0 + vl + kh * 5120);
            down_epilogue(acc, res, L_EX2, true, l == 1, WS_X0 + 64 * XT, WS_ST + 2 * 131072, WS_PH + PH_X2 * 2048 + c * 128 + j * 8, rs, lds0, out, c, j, wid,
                          lane, tag, tb_ + 22);
        } else if (wid_o == 4) {
            TS(1, tb_ + 8);
            poll(rs, ph + PH_H * 2048 + t * 8, tag, dead);
            TS(1, tb_ + 9);
#pragma unroll
            for (int i = 0; i < 11; ++i) BAR();
        } else {
            const u32 src = WS_P2 + jj * 276480;
            WAITV(40); TS(2, tb_ + 13); BAR();
            WAITV(30); TS(2, tb_ + 14); BAR();
#pragma unroll
            for (int m = 2; m < 6; ++m) {
                WAITV(20); BAR();
                dma_range<30>(rs, vl, src + (m + 3) * 30720, lds0 + (m - 2) * 30720, dw);
            }
            WAITV(20); TS(2, tb_ + 15); BAR();
            WAITV(10); TS(2, tb_ + 16); BAR();
            WAITV(0); TS(2, tb_ + 17); BAR();
            BAR(); BAR();
            if (l == 0) dma_range<PRE_Q>(rs, vl, WS_PQ + j * 163840, lds0, dw);
        }
        }
    }
    WAITV(0);
    if (b == 0 && tid == 256) put_flag(rs, WS_E, tag);
}

extern "C" void kernel_launch(void* const* d_in, const int* in_sizes, int n_in, void* d_out, int out_size, void* d_ws, size_t ws_size,
                              hipStream_t stream) {
    (void)in_sizes; (void)n_in; (void)out_size; (void)ws_size;
    (void)hipFuncSetAttribute((const void*)fused_layers, hipFuncAttributeMaxDynamicSharedMemorySize, LDS_BYTES);
    fused_layers<<<256, 512, LDS_BYTES, stream>>>((const u16*)d_in[0], (const u16*)d_in[1], (const u16*)d_in[2], (const u16*)d_in[4],
                                                  (const u16*)d_in[5], (const u16*)d_in[6], (const u16*)d_in[7], (u16*)d_out,
                                                  (unsigned char*)d_ws);
}
```

```cpp
#include <hip/hip_runtime.h>
#include <hip/hip_bf16.h>
#include <stdint.h>

#define LAS __attribute__((address_space(3)))
#define DI __device__ __forceinline__
typedef unsigned u32;
typedef unsigned short u16;
typedef unsigned long long u64;
typedef unsigned u32x2 __attribute__((ext_vector_type(2)));
typedef unsigned u32x4 __attribute__((ext_vector_type(4)));
typedef float f32x2 __attribute__((ext_vector_type(2)));
typedef float f32x4 __attribute__((ext_vector_type(4)));
typedef __bf16 bf16x2 __attribute__((ext_vector_type(2)));
typedef __bf16 bf16x8 __attribute__((ext_vector_type(8)));
typedef __amdgpu_buffer_rsrc_t rsrc_t;

#define PD_Q 5
#define PD_O 6
#define PD_1 5
#define PD_2 6
#define PRE_Q 114
#define PRE_1 87

constexpr u32 XT = 80 * 512;
constexpr u32 HT = 216 * 512;
constexpr u32 KVG = 5632;
constexpr u32 WS_E = 0;
constexpr u32 WS_F0 = 4096;
constexpr u32 WS_ST = WS_F0 + 256 * 512 + 131072;
constexpr u32 WS_CSQ = WS_ST + 4 * 131072;
constexpr u32 WS_CS1 = WS_CSQ + 16 * 20 * 512;
constexpr u32 WS_PQ = WS_CS1 + 16 * 20 * 512;
constexpr u32 WS_P1 = WS_PQ + 16 * 163840;
constexpr u32 WS_PO = WS_P1 + 16 * 143360;
constexpr u32 WS_P2 = WS_PO + 8 * 102400;
constexpr u32 WS_X0 = WS_P2 + 8 * 276480;
constexpr u32 WS_X1 = WS_X0 + 2 * 64 * XT;
constexpr u32 WS_O = WS_X1 + 2 * 64 * XT;
constexpr u32 WS_H = WS_O + 2 * 64 * XT;
constexpr u32 WS_KV = WS_H + 2 * 64 * HT;
constexpr u32 WS_END = WS_KV + 2 * 256 * 4 * KVG + 524288 + 4096;

constexpr u32 L_KV = 0;
constexpr u32 L_Q = 12 * KVG;
constexpr u32 L_EXO = 36864, L_EX2 = 122880;
constexpr u32 L_MISC = 153600;
constexpr u32 L_SA = L_MISC, L_SB = L_MISC + 512, L_CSQ = L_MISC + 1024, L_CS1 = L_MISC + 1536, L_B1 = L_MISC + 2048, L_B2 = L_MISC + 2560;
constexpr u32 L_WOA = 102400, L_WOB = 57344, L_WOC = L_MISC + 4096;
constexpr u32 LDS_BYTES = L_MISC + 10240;

#define PRE_AT 6
#define PZ_SLEEP 12
#define WIN_ 4
#define WG_ 2
#define NMAX 2
#define SEAM 2
#define SEAM_WAIT() __builtin_amdgcn_s_sleep(SEAM)
#define CODE_PF 2
template <int I> struct IC { static constexpr int v = I; };
template <int B, int E, class F> DI void sfor(F&& f) { if constexpr (B < E) { f(IC<B>{}); sfor<B + 1, E>(f); } }
DI u32 rfl(u32 x) { return __builtin_amdgcn_readfirstlane(x); }
DI f32x4 mfma(u32x4 a, u32x4 b, f32x4 c) {
    return __builtin_amdgcn_mfma_f32_16x16x32_bf16(__builtin_bit_cast(bf16x8, a), __builtin_bit_cast(bf16x8, b), c, 0, 0, 0);
}
DI u32 pk(float a, float b) { f32x2 v = {a, b}; return __builtin_bit_cast(u32, __builtin_convertvector(v, bf16x2)); }
DI float bflo(u32 w) { return __builtin_bit_cast(float, w << 16); }
DI float bfhi(u32 w) { return __builtin_bit_cast(float, w & 0xffff0000u); }
DI float f_of(u32 w) { return __builtin_bit_cast(float, w); }
DI u32 u_of(float f) { return __builtin_bit_cast(u32, f); }
#define SB() __builtin_amdgcn_sched_barrier(0)
#define BAR() asm volatile("s_barrier" ::: "memory")
#define LBAR() asm volatile("s_waitcnt lgkmcnt(0)\n\ts_barrier" ::: "memory")
#define PHASE_VARS() \
    int lane = lane_o, c = c_o, j = j_o, wid = wid_o; \
    asm volatile("" : "+v"(lane), "+s"(c), "+s"(j), "+s"(wid)); \
    const int t = lane & 15, g = lane >> 4, jj = j & 7, hf = j >> 3, dw = wid - 5; const u32 vl = lane * 16; \
    const u32 xin = WS_X0 + l * 64 * XT; \
    (void)t; (void)g; (void)jj; (void)hf; (void)dw; (void)vl; (void)xin;
#define WAITV(n) asm volatile("s_waitcnt vmcnt(%0)" :: "n"(n) : "memory")
#define WAITV0() do { asm volatile("" ::: "memory"); __builtin_amdgcn_s_waitcnt(0x0F70); asm volatile("" ::: "memory"); } while (0)
DI void dma16(rsrc_t rs, u32 voff, u32 soff, u32 ldsaddr) {
    asm volatile("s_mov_b32 m0, %0\n\ts_nop 0\n\tbuffer_load_dwordx4 %1, %2, %3 offen sc1 lds" :: "s"(ldsaddr), "v"(voff), "s"(rs), "s"(soff) : "memory");
}
template <int OFF> DI void gload(u32x4& d, rsrc_t rs, u32 voff, u32 soff) {
    asm volatile("buffer_load_dwordx4 %0, %1, %2, %3 offen offset:%4 sc1" : "=v"(d) : "v"(voff), "s"(rs), "s"(soff), "n"(OFF));
}
template <int OFF> DI void gload_again(u32x4& d, rsrc_t rs, u32 voff, u32 soff) {
    asm volatile("buffer_load_dwordx4 %0, %1, %2, %3 offen offset:%4 sc1" : "+v"(d) : "v"(voff), "s"(rs), "s"(soff), "n"(OFF));
}
template <int OFF> DI void lread(u32x4& d, u32 addr) { asm volatile("ds_read_b128 %0, %1 offset:%2" : "=v"(d) : "v"(addr), "n"(OFF)); }
template <int OFF> DI void lwrite(u32 addr, const u32x4& d) { asm volatile("ds_write_b128 %0, %1 offset:%2" :: "v"(addr), "v"(d), "n"(OFF) : "memory"); }
DI void st16(rsrc_t rs, u32 voff, u32 soff, u32x4 v) { __builtin_amdgcn_raw_buffer_store_b128(v, rs, voff, soff, 16); }
DI void st8(rsrc_t rs, u32 voff, u32 soff, u32x2 v) { __builtin_amdgcn_raw_buffer_store_b64(v, rs, voff, soff, 16); }
DI void st4(rsrc_t rs, u32 voff, u32 soff, u32 v) { __builtin_amdgcn_raw_buffer_store_b32(v, rs, voff, soff, 16); }
DI u32x4 ld16(rsrc_t rs, u32 voff, u32 soff) { return __builtin_amdgcn_raw_buffer_load_b128(rs, voff, soff, 16); }
DI u32x2 ld8(rsrc_t rs, u32 voff, u32 soff) { return __builtin_amdgcn_raw_buffer_load_b64(rs, voff, soff, 16); }
template <class T> DI T lds_ld(u32 a) { return *(const LAS T*)(uintptr_t)a; }
template <class T> DI void lds_st(u32 a, T v) { *(LAS T*)(uintptr_t)a = v; }
DI float rows_sum(float v) {
    auto a = __builtin_amdgcn_permlane32_swap(u_of(v), u_of(v), false, false); v = f_of(a[0]) + f_of(a[1]);
    auto b = __builtin_amdgcn_permlane16_swap(u_of(v), u_of(v), false, false); return f_of(b[0]) + f_of(b[1]);
}
DI float rows_max(float v) {
    auto a = __builtin_amdgcn_permlane32_swap(u_of(v), u_of(v), false, false); v = fmaxf(f_of(a[0]), f_of(a[1]));
    auto b = __builtin_amdgcn_permlane16_swap(u_of(v), u_of(v), false, false); return fmaxf(f_of(b[0]), f_of(b[1]));
}
DI u32x4 swap_pair(u32x2 p, u32x2 q) {
    auto a = __builtin_amdgcn_permlane16_swap(p.x, q.x, false, false);
    auto b = __builtin_amdgcn_permlane16_swap(p.y, q.y, false, false);
    return u32x4{a[0], b[0], a[1], b[1]};
}
DI u32x2 pk4(f32x4 v) { return u32x2{pk(v[0], v[1]), pk(v[2], v[3])}; }
DI f32x4 affine(f32x4 x, float a, f32x4 b) { return x * a + b; }
constexpr u32 POISON = 0xffffffffu;
DI u32 umax3(u32 a, u32 b, u32 c) { return max(max(a, b), c); }
DI u32 umax4(u32 m, u32x4 v) { return umax3(umax3(m, v.x, v.y), v.z, v.w); }
DI void poison_range(rsrc_t rs, u32 vl, u32 dst, int first, int step, int n) {
    const u32x4 p = {POISON, POISON, POISON, POISON};
    for (int i = first; i < n; i += step) st16(rs, vl, dst + i * 1024, p);
}

constexpr u32x4 POISON4 = {POISON, POISON, POISON, POISON};
DI void fill_x(rsrc_t rs, u32 vl, u32 data, u32 stat, int c, int j, int i) {
    if (i < 10) st16(rs, vl, data + (4 * c + 2 * (j >> 3) + (i & 1)) * XT + 10 * (j & 7) * 512 + (i >> 1) * 1024, POISON4);
    else st16(rs, vl < 512 ? vl : 0x80000000u, stat + c * 8192 + (j & 7) * 1024 + (j >> 3) * 512, POISON4);
}
DI void fill_o(rsrc_t rs, u32 vl, u32 data, int c, int j, int i) {
    st16(rs, (i >= 8 && vl >= 512) ? 0x80000000u : vl, data + (4 * c + (i & 3)) * XT + 5 * j * 512 + (i >> 2) * 1024, POISON4);
}
DI void fill_h(rsrc_t rs, u32 vl, u32 data, int c, int j, int i) {
    const int tb = (27 * j) >> 2, cnt = (j & 3) ? 7 : 6;
    st16(rs, (i >> 2) < cnt ? vl : 0x80000000u, data + (4 * c + (i & 3)) * HT + (tb + (i >> 2)) * 1024, POISON4);
}

DI void poll(rsrc_t rs, u32 voff, u64 tag, int& dead) {
    if (dead) return;
    for (int it = 0; it < (1 << 19); ++it) {
        u32x2 v;
        asm volatile("buffer_load_dwordx2 %0, %1, %2, 0 offen sc1\n\ts_waitcnt vmcnt(0)" : "=v"(v) : "v"(voff), "s"(rs) : "memory");
        if (__builtin_amdgcn_ballot_w64(v.x != (u32)tag || v.y != (u32)(tag >> 32)) == 0) return;
        __builtin_amdgcn_s_sleep(1);
    }
    dead = 1;
}
DI void put_flag(rsrc_t rs, u32 off, u64 tag) { st8(rs, off, 0, u32x2{(u32)tag, (u32)(tag >> 32)}); }
template <int N> DI void dma_range(rsrc_t rs, u32 vl, u32 src, u32 dst, int dw) {
    src += dw * 1024; dst += dw * 1024;
#pragma unroll
    for (int i = 0; i < (N + 2) / 3; ++i) {
        if (3 * i + 2 < N || 3 * i + dw < N) dma16(rs, vl, src + i * 3072, dst + i * 3072);
    }
}

struct QkvW { static constexpr int lin(int q) { return (((q >> 4) % 9) << 14) + (q & 15) * 1024; }
              static constexpr int seg(int q) { return lin(q) >> 16; }
              static constexpr int off(int q) { return lin(q) & 0xffff; }
              static constexpr bool bar(int q) { return q == 0 || q == 48 || q == PRE_Q || q == 144; }
              static constexpr int kstep(int s) { return s; } };
struct W1W { static constexpr int seg(int q) { return q >> 6; }
             static constexpr int off(int q) { return (q & 63) * 1024; }
             static constexpr bool bar(int q) { return q == 36 || q == PRE_1; }
             static constexpr int kstep(int s) { return s; } };
struct WoW { static constexpr int seg(int q) { return q < 25 ? 0 : (q < 44 ? 1 : 2); }
             static constexpr int off(int q) { return q < 25 ? (q / 5) * 10240 + (q % 5) * 1024 : (q - 25) * 1024; }
             static constexpr bool bar(int) { return false; }
             static constexpr int kstep(int s) { return s; } };
constexpr u64 pack5(int a, int b, int c, int d, int e, int f, int g, int h, int i) {
    return (u64)a | (u64)b << 5 | (u64)c << 10 | (u64)d << 15 | (u64)e << 20 | (u64)f << 25 | (u64)g << 30 | (u64)h << 35 | (u64)i << 40;
}
constexpr u64 W2O0 = pack5(0, 3, 10, 20, 7, 14, 17, 24, 1), W2O1 = pack5(4, 11, 21, 8, 15, 18, 25, 2, 5), W2O2 = pack5(12, 16, 22, 6, 9, 13, 19, 23, 26);
DI constexpr int w2_order(int s) { return (int)(((s < 9 ? W2O0 : (s < 18 ? W2O1 : W2O2)) >> (5 * (s < 9 ? s : (s < 18 ? s - 9 : s - 18)))) & 31); }
struct W2W { static constexpr int lin(int q) { return ((q / 15) % 5) * 30720 + ((q / 5) % 3) * 10240 + (q % 5) * 1024; }
             static constexpr int seg(int q) { return lin(q) >> 16; }
             static constexpr int off(int q) { return lin(q) & 0xffff; }
             static constexpr bool bar(int q) { return q % 15 == 0; }
             static constexpr int kstep(int s) { return w2_order(s); } };

constexpr int SPIN = 1 << 17;
template <int NSTEP, class W, int PD>
DI void gemm_preload(u32x4 (&xr)[NSTEP][2], rsrc_t rs, u32 vx0, u32 vx1, u32 xso) {
    sfor<0, PD>([&](auto I) { constexpr int s = I.v, k = W::kstep(s);
        gload<(k & 1) * 2048>(xr[s][0], rs, vx0, xso + (k >> 1) * 4096);
        gload<(k & 1) * 2048>(xr[s][1], rs, vx1, xso + (k >> 1) * 4096); });
}
template <int NT, int NSTEP, int NSWAP, class W, int PD, bool PRE>
DI void gemm_main(f32x4 (&acc)[NT][2], u32x4 (&xr)[NSTEP][2], rsrc_t rs, u32 vx0, u32 vx1, u32 xso, u32 la0, u32 la1, u32 la2, int& dead) {
    constexpr int WIN = WIN_, NQ = NT * NSTEP;
    u32x4 wf[NQ];
#pragma unroll
    for (int n = 0; n < NT; ++n) { acc[n][0] = f32x4{0.f, 0.f, 0.f, 0.f}; acc[n][1] = f32x4{0.f, 0.f, 0.f, 0.f}; }
    auto rldx = [&](auto I) { constexpr int s = I.v, k = W::kstep(s);
        gload_again<(k & 1) * 2048>(xr[s][0], rs, vx0, xso + (k >> 1) * 4096);
        gload_again<(k & 1) * 2048>(xr[s][1], rs, vx1, xso + (k >> 1) * 4096); };
    auto landed = [&](auto I, auto N) { constexpr int s = I.v;
        u32x4 &x0 = xr[s][0], &x1 = xr[s][1]; constexpr int n = N.v;
        asm volatile("s_waitcnt vmcnt(%2)" : "+v"(x0), "+v"(x1) : "n"(n)); };
    auto rdw = [&](auto I) { constexpr int q = I.v; constexpr int sg = W::seg(q);
        lread<W::off(q)>(wf[q], sg == 0 ? la0 : (sg == 1 ? la1 : la2)); };
    auto again = [&](auto S, auto L) { constexpr int s = S.v, last = L.v;
#pragma unroll 1
        for (int it = 0;; ++it) {
            sfor<s, last + 1>([&](auto I) { rldx(I); });
            landed(S, IC<2 * (last - s)>{});
            if (__builtin_amdgcn_ballot_w64(umax4(umax4(0u, xr[s][0]), xr[s][1]) == POISON) == 0) break;
            if (it == SPIN) { dead = 1; break; }
        } };
    if constexpr (!PRE) gemm_preload<NSTEP, W, PD>(xr, rs, vx0, vx1, xso);
    landed(IC<0>{}, IC<2 * (PD - 1)>{});
    if (__builtin_amdgcn_ballot_w64(umax4(umax4(0u, xr[0][0]), xr[0][1]) == POISON) != 0 && !dead) again(IC<0>{}, IC<PD - 1>{});
    if constexpr (W::bar(0)) BAR();
    sfor<0, WIN>([&](auto I) { rdw(I); });
    u32 m = 0;
    u64 bad = 0;
    sfor<0, NSTEP>([&](auto I) { constexpr int s = I.v;
        constexpr bool LD = s + PD < NSTEP;
        constexpr int last = LD ? s + PD : NSTEP - 1;
        constexpr bool CK = s + 1 < NSTEP;
        constexpr int sn = CK ? s + 1 : s;
        auto extra = [&](auto G) { constexpr int k = G.v;
            constexpr int kn = W::kstep(LD ? s + PD : 0);
            if constexpr (LD && k == 0) gload<(kn & 1) * 2048>(xr[LD ? s + PD : 0][0], rs, vx0, xso + (kn >> 1) * 4096);
            if constexpr (LD && k == 1) gload<(kn & 1) * 2048>(xr[LD ? s + PD : 0][1], rs, vx1, xso + (kn >> 1) * 4096);
            if constexpr (CK && k == 2) landed(IC<sn>{}, IC<2 * (last - sn)>{});
            if constexpr (CK && k >= 3 && k < 3 + NMAX) {
                constexpr int mi = k - 3;
                const u32x4& x = xr[sn][NMAX == 4 ? mi >> 1 : mi];
                const u32 p = NMAX == 4 ? ((mi & 1) ? x.x : x.z) : x.x, q = NMAX == 4 ? ((mi & 1) ? x.y : x.w) : x.z;
                asm volatile("v_max3_u32 %0, %0, %1, %2" : "+v"(m) : "v"(p), "v"(q));
            }
            if constexpr (CK && k == 3 + NMAX) { bad = __builtin_amdgcn_ballot_w64(m == POISON); asm volatile("" : "+s"(bad)); }
            if constexpr (CK && k == 5 + NMAX) {
                if (__builtin_expect(bad != 0, 0)) {
                    asm volatile("" ::: "memory");
                    if (!dead) again(IC<sn>{}, IC<last>{});
                    m = 0;
                }
            }
            SB(); };
        SB();
        sfor<0, NT>([&](auto J) { constexpr int n = J.v; constexpr int q = s * NT + n, qn = q + WIN;
            if constexpr (qn < NQ && W::bar(qn)) BAR();
            if constexpr (q % WG_ == 0) {
                constexpr int issued = q + WIN - 1 < NQ - 1 ? q + WIN - 1 : NQ - 1, need = q + WG_ - 1 < NQ - 1 ? q + WG_ - 1 : NQ - 1;
                asm volatile("s_waitcnt lgkmcnt(%0)" :: "n"(issued - need)); SB();
            }
            if constexpr (n < NSWAP) acc[n][0] = mfma(wf[q], xr[s][0], acc[n][0]); else acc[n][0] = mfma(xr[s][0], wf[q], acc[n][0]);
            SB();
            extra(IC<2 * n>{});
            if constexpr (qn < NQ) { rdw(IC<qn>{}); SB(); }
            if constexpr (n < NSWAP) acc[n][1] = mfma(wf[q], xr[s][1], acc[n][1]); else acc[n][1] = mfma(xr[s][1], wf[q], acc[n][1]);
            SB();
            extra(IC<2 * n + 1>{});
        });
    });
#pragma unroll
    for (int n = 0; n < NT; ++n) asm volatile("" : "+v"(acc[n][0]), "+v"(acc[n][1]));
}

struct StatRegs { u32x4 v[8]; };
template <int NP> DI void stat_load(StatRegs& r, rsrc_t rs, u32 base, int lane) {
#pragma unroll
    for (int p = 0; p < NP; ++p) r.v[p] = ld16(rs, lane * 16, base + p * 1024);
}
template <int NP> DI void stat_check(StatRegs& r, rsrc_t rs, u32 base, int lane, int& dead) {
#pragma unroll 1
    for (int it = 0;; ++it) {
        u32 m = 0;
#pragma unroll
        for (int p = 0; p < NP; ++p) m = umax4(m, r.v[p]);
        if (dead || __builtin_amdgcn_ballot_w64(m == POISON) == 0) break;
        if (it == SPIN) dead = 1;
        asm volatile("" ::: "memory");
        stat_load<NP>(r, rs, base, lane);
    }
}
template <int NP, bool TWICE> DI void stat_finish(const StatRegs& r, u32 lds0, int lane) {
    float s0 = 0.f, q0 = 0.f, s1 = 0.f, q1 = 0.f;
#pragma unroll
    for (int p = 0; p < NP; ++p) { s0 += f_of(r.v[p].x); q0 += f_of(r.v[p].y); s1 += f_of(r.v[p].z); q1 += f_of(r.v[p].w); }
    auto fin = [&](float s, float q, float& A, float& B) {
        const float m = s * (1.f / 640.f);
        const float v = fmaxf(q * (1.f / 640.f) - m * m, 0.f);
        float r1 = 1.f / sqrtf(v + 1e-5f);
        if (TWICE) r1 *= 1.f / sqrtf(v * r1 * r1 + 1e-5f);
        A = r1; B = -m * r1; };
    float A0, B0, A1, B1;
    fin(s0, q0, A0, B0); fin(s1, q1, A1, B1);
    lds_st<f32x2>(lds0 + L_SA + lane * 8, f32x2{A0, A1});
    lds_st<f32x2>(lds0 + L_SB + lane * 8, f32x2{B0, B1});
}

DI int qkv_col(int j, int n, int f) {
    const int a = 40 * j;
    if (n < 4) return (n >> 1) * 640 + a + (n & 1) * 20 + f;
    if (n == 4) return (f >> 3) * 640 + a + ((f >> 2) & 1) * 20 + 16 + (f & 3);
    if (n < 7) return 1280 + a + (n - 5) * 20 + f;
    return f < 8 ? 1280 + a + (f >> 2) * 20 + 16 + (f & 3) : -1;
}
DI void start_flag(rsrc_t rs, u32 base, int c, int j, int lane, u64 tag) {
    const int q = lane & 15, k = lane >> 4;
    const int cc = k == 0 ? c : q, cj = k == 0 ? q : (k == 1 ? j : (j ^ 8)), slot = k == 0 ? j : 16 * k + c;
    put_flag(rs, base + (cc * 16 + cj) * 512 + slot * 8, tag);
}
DI void prologue(const u16* x, const u16* wqkv, const u16* wo, const u16* w1, const u16* w2, rsrc_t rs, u32 lds0, int c, int j, int wid,
                 int lane, int tid, u64 tag) {
    const int f = lane & 15, g = lane >> 4;
    const int tok = 128 * c + 8 * j + wid;
    const rsrc_t rx = __builtin_amdgcn_make_buffer_rsrc((void*)x, 0, 2048 * 1280, 0x00020000);
    const u32x4 xa = __builtin_amdgcn_raw_buffer_load_b128(rx, lane * 16, tok * 1280, 0);
    const u32x4 xb = __builtin_amdgcn_raw_buffer_load_b128(rx, lane < 16 ? 1024 + lane * 16 : 0x80000000u, tok * 1280, 0);
    u16 wv[4][8]; u32 dst[4], csd[4]; bool ok[4];
#pragma unroll
    for (int i = 0; i < 4; ++i) {
        const int e = wid + 8 * i;
        const u16* src = w2; u32 bytes = 1728 * 640 * 2; int ld = 640, rA = 0, rB = 0, col = -1; u32 d = 0, cs = 0; bool valid = e < 31;
        if (e < 10) {
            const int p = 10 * c + e, s = p >> 3, n = p & 7;
            src = wqkv; bytes = 640 * 1920 * 2; ld = 1920; rA = 32 * s + 8 * g; rB = rA + 4; col = qkv_col(j, n, f);
            d = WS_PQ + j * 163840 + p * 1024; cs = WS_CSQ + (j * 20 + s) * 512 + n * 64;
        } else if (e < 19) {
            const int p = c + 16 * (e - 10), s = p / 7, n = p % 7, tb = (27 * j) >> 2, cnt = (j & 3) ? 7 : 6;
            valid = p < 140;
            src = w1; bytes = 640 * 1728 * 2; ld = 1728; rA = 32 * s + 8 * g; rB = rA + 4; col = n < cnt ? 16 * (tb + n) + f : -1;
            d = WS_P1 + j * 143360 + p * 1024; cs = WS_CS1 + (j * 20 + s) * 512 + n * 64;
        } else {
            const int p = 2 * c + (j >> 3) + 32 * (e - 19), jj = j & 7;
            valid = valid && p < 370;
            if (p < 100) {
                const int it = p / 10, kh = (p / 5) & 1, n = p % 5, U = 4 * (kh * 10 + it) + g, jp = U / 5, u = U % 5;
                src = wo; bytes = 640 * 640 * 2;
                rA = 40 * jp + (u == 4 ? 16 : (u >> 1) * 20 + (u & 1) * 8); rB = u == 4 ? 40 * jp + 36 : rA + 4;
                col = 80 * jj + 16 * n + f; d = WS_PO + jj * 102400 + (it < 5 ? p : 50 + kh * 25 + (it - 5) * 5 + n) * 1024;
            } else {
                const int p2 = p - 100, it = p2 / 10, kh = (p2 / 5) & 1, n = p2 % 5;
                rA = 32 * (kh * 27 + w2_order(it)) + 8 * g; rB = rA + 4; col = 80 * jj + 16 * n + f; d = WS_P2 + jj * 276480 + p2 * 1024;
            }
        }
        const rsrc_t rw = __builtin_amdgcn_make_buffer_rsrc((void*)src, 0, bytes, 0x00020000);
        const bool live = valid && col >= 0;
        const u32 vA = live ? (u32)(rA * ld + col) * 2 : 0x80000000u, vB = live ? (u32)(rB * ld + col) * 2 : 0x80000000u;
#pragma unroll
        for (int k = 0; k < 4; ++k) {
            wv[i][k] = __builtin_amdgcn_raw_buffer_load_b16(rw, vA, k * ld * 2, 0);
            wv[i][4 + k] = __builtin_amdgcn_raw_buffer_load_b16(rw, vB, k * ld * 2, 0);
        }
        dst[i] = rfl(d); csd[i] = rfl(cs); ok[i] = valid;
    }
    poison_range(rs, lane * 16, WS_KV + (c * 16 + j) * 4 * KVG, wid, 8, 22);
    fill_o(rs, lane * 16, WS_O, c, j, wid);
    if (wid < 4) fill_o(rs, lane * 16, WS_O, c, j, 8 + wid);
    {
        float s = 0.f, q = 0.f;
#pragma unroll
        for (int k = 0; k < 4; ++k) {
            const float a0 = bflo(xa[k]), a1 = bfhi(xa[k]), b0 = bflo(xb[k]), b1 = bfhi(xb[k]);
            s += a0 + a1 + b0 + b1; q += a0 * a0 + a1 * a1 + b0 * b0 + b1 * b1;
        }
#pragma unroll
        for (int o = 1; o < 16; o <<= 1) { s += __shfl_xor(s, o); q += __shfl_xor(q, o); }
        s = rows_sum(s); q = rows_sum(q);
        if (lane == 0) st8(rs, 0, WS_ST + c * 8192 + (8 * j + wid) * 8, u32x2{u_of(s), u_of(q)});
        lds_st<u32x4>(lds0 + lane * 144 + wid * 16, xa);
        if (lane < 16) lds_st<u32x4>(lds0 + (64 + lane) * 144 + wid * 16, xb);
        LBAR();
        const u32 base = WS_X0 + (4 * c + (j >> 2)) * XT + (j & 3) * 128;
        { const int q0 = tid; st16(rs, (q0 >> 3) * 512 + (q0 & 7) * 16, base, lds_ld<u32x4>(lds0 + (q0 >> 3) * 144 + (q0 & 7) * 16)); }
        if (tid < 128) { const int q0 = tid + 512; st16(rs, (q0 >> 3) * 512 + (q0 & 7) * 16, base, lds_ld<u32x4>(lds0 + (q0 >> 3) * 144 + (q0 & 7) * 16)); }
    }
    auto put = [&](int i) {
        u32x4 v;
#pragma unroll
        for (int k = 0; k < 4; ++k) v[k] = (u32)wv[i][2 * k] | ((u32)wv[i][2 * k + 1] << 16);
        st16(rs, lane * 16, dst[i], v);
        if (csd[i]) {
            float s = 0.f;
#pragma unroll
            for (int k = 0; k < 4; ++k) s += bflo(v[k]) + bfhi(v[k]);
            s = rows_sum(s);
            if (lane < 16) st4(rs, lane * 4, csd[i], u_of(s));
        }
    };
#pragma unroll
    for (int i = 0; i < 4; ++i) if (ok[i]) put(i);
    WAITV0();
    LBAR();
    if (tid < 48) start_flag(rs, WS_F0, c, j, lane, tag);
}

struct Att { f32x4 nm[2], o[2], orr[2]; };
constexpr float NEG = -3.0e38f, RAISE = 24.f;
DI float max8(float m, f32x4 a, f32x4 b) {
    m = fmaxf(fmaxf(m, a[0]), a[1]); m = fmaxf(fmaxf(m, a[2]), a[3]);
    m = fmaxf(fmaxf(m, b[0]), b[1]);
    return fmaxf(fmaxf(m, b[2]), b[3]);
}
DI float ex2(float v) { return __builtin_amdgcn_exp2f(v); }
DI void att_check(f32x4 (&s)[2][2], Att& a) {
    if (__builtin_expect(__builtin_amdgcn_ballot_w64(max8(max8(NEG, s[0][0], s[1][0]), s[0][1], s[1][1]) > RAISE) != 0, 0)) {
#pragma unroll
        for (int q = 0; q < 2; ++q) {
            const float d = rows_max(max8(0.f, s[0][q], s[1][q])), sc = ex2(-d);
            s[0][q] -= d; s[1][q] -= d; a.nm[q] -= d; a.o[q] *= sc; a.orr[q] *= sc;
        }
    }
}
DI void ldk(u32x4 (&k)[2], u32 a, u32 kd) {
    const u32 r = a + kd;
    const u32x2 m0 = lds_ld<u32x2>(a), r0 = lds_ld<u32x2>(r);
    asm volatile("" ::: "memory");
    const u32x2 m1 = lds_ld<u32x2>(a + 8), r1 = lds_ld<u32x2>(r + 8);
    k[0] = u32x4{m0.x, m0.y, r0.x, r0.y}; k[1] = u32x4{m1.x, m1.y, r1.x, r1.y};
}
DI void ldv(u32x4 (&v)[2], u32 a, int hd) { v[0] = lds_ld<u32x4>(a + 2560); v[1] = lds_ld<u32x4>(a + 4608 - hd * 1024); }
template <bool NEXT>
DI void att_step(const f32x4 (&sc)[2][2], f32x4 (&sn)[2][2], const u32x4 (&kn)[2], const u32x4 (&vc)[2], u32x4 (&kl)[2], u32x4 (&vn)[2], u32 ka, u32 va,
                 int hd, u32 kd, const u32x4 (&qf)[2], Att& a) {
    float e[8], f[8];
    u32x4 p, p2;
    u64 bad = 0;
    SB();
    if (NEXT) sn[0][0] = mfma(kn[0], qf[0], a.nm[0]);
    e[0] = ex2(sc[0][0][0]); e[1] = ex2(sc[0][0][1]); SB();
    if (NEXT) sn[1][0] = mfma(kn[1], qf[0], a.nm[0]);
    e[2] = ex2(sc[0][0][2]); e[3] = ex2(sc[0][0][3]); SB();
    if (NEXT) sn[0][1] = mfma(kn[0], qf[1], a.nm[1]);
    e[4] = ex2(sc[1][0][0]); e[5] = ex2(sc[1][0][1]); SB();
    if (NEXT) sn[1][1] = mfma(kn[1], qf[1], a.nm[1]);
    e[6] = ex2(sc[1][0][2]); e[7] = ex2(sc[1][0][3]); SB();
    if (NEXT) ldk(kl, ka, kd);
    f[0] = ex2(sc[0][1][0]);
    p = u32x4{pk(e[0], e[1]), pk(e[2], e[3]), pk(e[4], e[5]), pk(e[6], e[7])};
    f[1] = ex2(sc[0][1][1]); SB();
    a.o[0] = mfma(vc[0], p, a.o[0]);
    f[2] = ex2(sc[0][1][2]); f[3] = ex2(sc[0][1][3]); SB();
    a.orr[0] = mfma(vc[1], p, a.orr[0]);
    float mx = NEG;
    if (NEXT) {
        mx = max8(max8(mx, sn[0][0], sn[1][0]), sn[0][1], sn[1][1]);
        bad = __builtin_amdgcn_ballot_w64(mx > RAISE);
        asm volatile("" : "+s"(bad));
    }
    SB();
    f[4] = ex2(sc[1][1][0]); f[5] = ex2(sc[1][1][1]); f[6] = ex2(sc[1][1][2]); f[7] = ex2(sc[1][1][3]); SB();
    if (NEXT) ldv(vn, va, hd);
    p2 = u32x4{pk(f[0], f[1]), pk(f[2], f[3]), pk(f[4], f[5]), pk(f[6], f[7])}; SB();
    a.o[1] = mfma(vc[0], p2, a.o[1]);
    a.orr[1] = mfma(vc[1], p2, a.orr[1]);
    if (NEXT) {
        if (__builtin_expect(bad != 0, 0)) {
#pragma unroll
            for (int q = 0; q < 2; ++q) {
                const float d = rows_max(max8(0.f, sn[0][q], sn[1][q])), scl = ex2(-d);
                sn[0][q] -= d; sn[1][q] -= d; a.nm[q] -= d; a.o[q] *= scl; a.orr[q] *= scl;
            }
        }
    }
}
template <int MODE, bool INIT>
DI void att_chunk(u32 a0, u32 a1, int nfull, int hd, u32 kd, const u32x4 (&qf)[2], Att& a, int t, int g) {
    u32x4 kA[2], kB[2], vA[2], vB[2];
    f32x4 sA[2][2], sB[2][2];
    ldk(kA, a0, kd);
    ldk(kB, a1, kd);
    asm volatile("" ::: "memory");
    ldv(vA, a0, hd);
    const f32x4 ng = {NEG, NEG, NEG, NEG};
    sA[0][0] = mfma(kA[0], qf[0], a.nm[0]);
    sA[1][0] = MODE == 1 ? ng : mfma(kA[1], qf[0], a.nm[0]);
    sA[0][1] = MODE == 2 ? ng : mfma(kA[0], qf[1], a.nm[1]);
    sA[1][1] = mfma(kA[1], qf[1], a.nm[1]);
    if (MODE != 0) {
#pragma unroll
        for (int r = 0; r < 4; ++r) {
            const bool keep = MODE == 1 ? (4 * g + r <= t) : (4 * g + r > t);
            sA[0][0][r] = keep ? sA[0][0][r] : NEG; sA[1][1][r] = keep ? sA[1][1][r] : NEG;
        }
    }
    if (INIT) {
#pragma unroll
        for (int q = 0; q < 2; ++q) {
            const float d = rows_max(max8(NEG, sA[0][q], sA[1][q]));
            sA[0][q] -= d; sA[1][q] -= d; a.nm[q] = f32x4{-d, -d, -d, -d};
        }
    } else att_check(sA, a);
    u32 ka = a1 + KVG, va = a1;
    int rem = nfull;
#pragma unroll 1
    while (true) {
        if (rem == 0) { att_step<false>(sA, sB, kB, vA, kA, vB, ka, va, hd, kd, qf, a); break; }
        att_step<true>(sA, sB, kB, vA, kA, vB, ka, va, hd, kd, qf, a);
        ka += KVG; va += KVG; --rem;
        if (rem == 0) { att_step<false>(sB, sA, kA, vB, kB, vA, ka, va, hd, kd, qf, a); break; }
        att_step<true>(sB, sA, kA, vB, kB, vA, ka, va, hd, kd, qf, a);
        ka += KVG; va += KVG; --rem;
    }
}

struct GeluK { f32x2 c0, c1, one; };
DI GeluK gelu_consts() {
    constexpr float C0 = -2.f * 0.7978845608028654f * 1.4426950408889634f, C1 = C0 * 0.044715f;
    GeluK k = {f32x2{C0, C0}, f32x2{C1, C1}, f32x2{1.f, 1.f}};
    asm volatile("" : "+v"(k.c0), "+v"(k.c1), "+v"(k.one));
    return k;
}
DI u32x4 gelu_tile(const f32x4& a0, const f32x4& a1, const float (&sA)[2], const float (&sB)[2], const GeluK& k, u32 lds0, int n, int g) {
    const f32x4 cs = lds_ld<f32x4>(lds0 + L_CS1 + (16 * n + 4 * g) * 4), bs = lds_ld<f32x4>(lds0 + L_B1 + (16 * n + 4 * g) * 4);
    f32x2 y[4], e[4];
#pragma unroll
    for (int i = 0; i < 4; ++i) {
        const int u = i >> 1, h = i & 1;
        const f32x4& a = u ? a1 : a0;
        const f32x2 av = {a[2 * h], a[2 * h + 1]}, cv = {cs[2 * h], cs[2 * h + 1]}, bv = {bs[2 * h], bs[2 * h + 1]};
        y[i] = av * sA[u] + (cv * sB[u] + bv);
    }
#pragma unroll
    for (int i = 0; i < 4; ++i) e[i] = y[i] * ((y[i] * y[i]) * k.c1 + k.c0);
#pragma unroll
    for (int i = 0; i < 4; ++i) e[i] = f32x2{__builtin_amdgcn_exp2f(e[i][0]), __builtin_amdgcn_exp2f(e[i][1])};
#pragma unroll
    for (int i = 0; i < 4; ++i) e[i] += k.one;
#pragma unroll
    for (int i = 0; i < 4; ++i) e[i] = f32x2{__builtin_amdgcn_rcpf(e[i][0]), __builtin_amdgcn_rcpf(e[i][1])};
#pragma unroll
    for (int i = 0; i < 4; ++i) y[i] *= e[i];
    return swap_pair(u32x2{pk(y[0][0], y[0][1]), pk(y[1][0], y[1][1])}, u32x2{pk(y[2][0], y[2][1]), pk(y[3][0], y[3][1])});
}

template <class F> DI void gelu_all(const f32x4 (&acc)[7][2], rsrc_t rs, u32 lds0, int l, int c, int j, int w, int lane, F&& next) {
    const int t = lane & 15, g = lane >> 4;
    float sA[2], sB[2];
#pragma unroll
    for (int u = 0; u < 2; ++u) {
        sA[u] = lds_ld<float>(lds0 + L_SA + (32 * w + 16 * u + t) * 4); sB[u] = lds_ld<float>(lds0 + L_SB + (32 * w + 16 * u + t) * 4);
    }
    const int tb = (27 * j) >> 2, cnt = (j & 3) ? 7 : 6;
    const u32 hb = WS_H + (l * 64 + 4 * c + w) * HT + tb * 1024;
    const GeluK gk = gelu_consts();
    sfor<0, 7>([&](auto N) { constexpr int n = N.v;
        if (n < 6 || cnt == 7) st16(rs, lane * 16, hb + n * 1024, gelu_tile(acc[n][0], acc[n][1], sA, sB, gk, lds0, n, g));
        if constexpr (n == PRE_AT) { SB(); next(); SB(); }
    });
}

DI void down_epilogue(f32x4 (&acc)[5][2], const u32x2 (&res)[5], u32 lex, bool bias, bool last, u32 dstx, u32 stbuf, rsrc_t rs, u32 lds0,
                      u16* out, int c, int j, int tile, int wid, int lane) {
    const int t = lane & 15, g = lane >> 4, jj = j & 7, hf = j >> 3, tt = wid & 1, kh = wid >> 1; const u32 vl = lane * 16;
#pragma unroll
    for (int n = 0; n < 5; ++n) lds_st<f32x4>(lds0 + lex + wid * 5120 + n * 1024 + vl, acc[n][1]);
    LBAR();
    f32x4 v[5];
    float s = 0.f, q = 0.f;
#pragma unroll
    for (int n = 0; n < 5; ++n) {
        v[n] = acc[n][0] + lds_ld<f32x4>(lds0 + lex + (wid ^ 2) * 5120 + n * 1024 + vl);
        v[n] += f32x4{bflo(res[n].x), bfhi(res[n].x), bflo(res[n].y), bfhi(res[n].y)};
        if (bias) v[n] += lds_ld<f32x4>(lds0 + L_B2 + (16 * n + 4 * g) * 4);
#pragma unroll
        for (int r = 0; r < 4; ++r) { s += v[n][r]; q += v[n][r] * v[n][r]; }
    }
    u32x2 p[5];
#pragma unroll
    for (int n = 0; n < 5; ++n) p[n] = pk4(v[n]);
    const u32x4 c01 = swap_pair(p[0], p[1]), c23 = swap_pair(p[2], p[3]), c44 = swap_pair(p[4], p[4]);
    const int T = 16 * kh + t;
    if (!last) {
        const u32 base = dstx + tile * XT + 10 * jj * 512;
        const u32 vo = (2 * (g & 1) + (g >> 1)) * 512 + T * 16;
        st16(rs, vo, base, c01);
        st16(rs, vo, base + 2048, c23);
        if ((g & 1) == 0) st16(rs, (g >> 1) * 512 + T * 16, base + 4096, c44);
        s = rows_sum(s); q = rows_sum(q);
        if (g == 0) st8(rs, (64 * hf + 32 * tt + T) * 8, stbuf + c * 8192 + jj * 1024, u32x2{u_of(s), u_of(q)});
    } else {
        const rsrc_t ro = __builtin_amdgcn_make_buffer_rsrc((void*)out, 0, 2048 * 1280, 0x00020000);
        const u32 vo = T * 1280 + (16 * (g & 1) + 8 * (g >> 1)) * 2, so = tile * 32 * 1280 + 160 * jj;
        __builtin_amdgcn_raw_buffer_store_b128(c01, ro, vo, so, 0);
        __builtin_amdgcn_raw_buffer_store_b128(c23, ro, vo + 64, so, 0);
        if ((g & 1) == 0) __builtin_amdgcn_raw_buffer_store_b128(c44, ro, vo + 128, so, 0);
    }
    LBAR();
    if (!last) SEAM_WAIT();
}
DI void res_load(u32x2 (&res)[5], u32 src, rsrc_t rs, int tile, int j, int wid, int lane) {
    const int t = lane & 15, g = lane >> 4, jj = j & 7, kh = wid >> 1;
    const u32 base = src + tile * XT + 10 * jj * 512;
#pragma unroll
    for (int n = 0; n < 5; ++n) res[n] = ld8(rs, (g >> 1) * 512 + (16 * kh + t) * 16 + (g & 1) * 8, base + n * 1024);
}

__global__ __launch_bounds__(512, 2) void fused_layers(const u16* __restrict__ x, const u16* __restrict__ wqkv, const u16* __restrict__ wo,
                                                       const u16* __restrict__ w1, const u16* __restrict__ b1, const u16* __restrict__ w2,
                                                       const u16* __restrict__ b2, u16* __restrict__ out, unsigned char* __restrict__ ws) {
    const int tid = threadIdx.x, lane_o = tid & 63, wid_o = rfl(tid >> 6);
    const int b = blockIdx.x;
    const int c_o = 8 * (b & 1) + (b >> 5), j_o = 4 * ((b >> 1) & 3) + ((b >> 3) & 3);
    constexpr u32 lds0 = 0;
    const rsrc_t rs = __builtin_amdgcn_make_buffer_rsrc((void*)ws, 0, WS_END, 0x00020000);
    {
        u64 pc; u32 len;
        asm volatile("s_getpc_b64 %0\n.Lcode_first:\n\ts_mov_b32 %1, .Lcode_last-.Lcode_first" : "=s"(pc), "=s"(len));
        const rsrc_t rc = __builtin_amdgcn_make_buffer_rsrc((void*)pc, 0, len, 0x00020000);
        if (wid_o >= 8 - CODE_PF) {
            const u32 k = (b >> 3) * CODE_PF + (wid_o - (8 - CODE_PF));
            const u32 off = (k % ((len + 8191) >> 13)) * 8192;
            asm volatile("s_mov_b32 m0, %0\n\ts_nop 0\n\tbuffer_load_dword %1, %2, %3 offen lds" :: "s"(lds0 + L_SA), "v"(lane_o * 128), "s"(rc), "s"(off) : "memory");
        }
    }
    u64 tag;
    {
        const u32x2 e = ld8(rs, 0, WS_E);
        tag = (((u64)rfl(e.y) << 32) | rfl(e.x)) + 1;
    }
    int dead = 0;
    prologue(x, wqkv, wo, w1, w2, rs, lds0, c_o, j_o, wid_o, lane_o, tid, tag);

#pragma unroll 1
    for (int l = 0; l < 2; ++l) {
        { PHASE_VARS();
        if (wid_o < 4) {
            f32x4 acc[8][2];
            if (l == 0) { WAITV0(); BAR(); }
            const u32 vx = g * 512 + t * 16;
            u32x4 xr[20][2];
            gemm_main<8, 20, 5, QkvW, PD_Q, false>(acc, xr, rs, vx, vx + 256, xin + (4 * c + wid) * XT, lds0 + vl, lds0 + vl + 65536, lds0 + vl + 131072, dead);
            BAR();
            constexpr float QS = 0.22360679774997896f * 1.4426950408889634f;
            float sA[2], sB[2]; f32x4 A4[2], B4[2];
#pragma unroll
            for (int u = 0; u < 2; ++u) {
                sA[u] = lds_ld<float>(lds0 + L_SA + (32 * wid + 16 * u + t) * 4); sB[u] = lds_ld<float>(lds0 + L_SB + (32 * wid + 16 * u + t) * 4);
                A4[u] = lds_ld<f32x4>(lds0 + L_SA + (32 * wid + 16 * u + 4 * g) * 4); B4[u] = lds_ld<f32x4>(lds0 + L_SB + (32 * wid + 16 * u + 4 * g) * 4);
            }
            f32x4 cst[5]; float csv[3];
#pragma unroll
            for (int n = 0; n < 5; ++n) cst[n] = lds_ld<f32x4>(lds0 + L_CSQ + (16 * n + 4 * g) * 4);
#pragma unroll
            for (int n = 0; n < 3; ++n) csv[n] = lds_ld<float>(lds0 + L_CSQ + (16 * (5 + n) + t) * 4);
            SB();
            const u32 kvl = lds0 + L_KV + (8 + wid) * KVG + vl, ql = lds0 + L_Q + wid * 4096 + vl;
            const u32 kvg = WS_KV + (l * 256 + c * 16 + j) * 4 * KVG + wid * KVG;
            auto fin_t = [&](auto N, int u, float sc) { constexpr int n = N.v;
                return pk4(affine(acc[n][u], sA[u] * sc, cst[n] * (sB[u] * sc))); };
            u32x2 rem[2];
#pragma unroll
            for (int u = 0; u < 2; ++u) rem[u] = fin_t(IC<4>{}, u, g < 2 ? QS : 1.f);
            sfor<0, 2>([&](auto H) { constexpr int hd = H.v;
                const u32x2 m0 = fin_t(IC<2 + hd>{}, 0, 1.f), m1 = fin_t(IC<2 + hd>{}, 1, 1.f);
                const u32x4 kfr = {m0.x, m0.y, m1.x, m1.y};
                st16(rs, vl, kvg + hd * 1024, kfr);
                lds_st<u32x4>(kvl + hd * 1024, kfr);
            });
            {
                const u32x4 kfr = {rem[0].x, rem[0].y, rem[1].x, rem[1].y};
                st16(rs, g >= 2 ? vl - 512 : 0x80000000u, kvg + 2048, kfr);
                if (g >= 2) lds_st<u32x4>(kvl + 2048 - 512, kfr);
            }
            sfor<5, 8>([&](auto N) { constexpr int n = N.v;
                const float cs = csv[n - 5];
                u32x2 m[2];
#pragma unroll
                for (int u = 0; u < 2; ++u) {
                    m[u] = pk4(A4[u] * acc[n][u] + B4[u] * cs);
                    if (n == 7) m[u] = t == 8 ? u32x2{0x3f803f80u, 0x3f803f80u} : m[u];
                }
                const u32x4 vfr = {m[0].x, m[0].y, m[1].x, m[1].y};
                st16(rs, vl, kvg + 2560 + (n - 5) * 1024, vfr);
                lds_st<u32x4>(kvl + 2560 + (n - 5) * 1024, vfr);
            });
#pragma unroll
            for (int u = 0; u < 2; ++u) {
                sfor<0, 2>([&](auto H) { constexpr int hd = H.v;
                    const u32x2 m = fin_t(IC<hd>{}, u, QS);
                    const bool own = g == hd;
                    lds_st<u32x4>(ql + (hd * 2 + u) * 1024, u32x4{m.x, m.y, own ? rem[u].x : 0u, own ? rem[u].y : 0u});
                });
            }
        } else if (wid_o == 4) {
            if (l == 0) {
                poll(rs, WS_F0 + (c * 16 + j) * 512 + (lane < 48 ? lane : lane - 16) * 8, tag, dead);
                BAR();
            }
            BAR();
            StatRegs sr;
            if (l == 0) {
                stat_load<1>(sr, rs, WS_ST + c * 8192, lane);
                u32x4 cq[10];
                const int qd = lane & 31, hs = lane >> 5;
#pragma unroll
                for (int i = 0; i < 10; ++i) cq[i] = ld16(rs, (hs * 10 + i) * 512 + qd * 16, WS_CSQ + j * 10240);
                const int tb = (27 * j) >> 2, cnt = (j & 3) ? 7 : 6;
                const rsrc_t rb1 = __builtin_amdgcn_make_buffer_rsrc((void*)b1, 0, 1728 * 2, 0x00020000);
                const rsrc_t rb2 = __builtin_amdgcn_make_buffer_rsrc((void*)b2, 0, 640 * 2, 0x00020000);
                const u16 bb0 = __builtin_amdgcn_raw_buffer_load_b16(rb1, (16 * tb + lane) * 2, 0, 0);
                const u16 bb1 = __builtin_amdgcn_raw_buffer_load_b16(rb1, lane + 64 < 16 * cnt ? (16 * tb + lane + 64) * 2 : 0x80000000u, 0, 0);
                const u16 c0 = __builtin_amdgcn_raw_buffer_load_b16(rb2, (80 * jj + lane) * 2, 0, 0);
                const u16 c1b = __builtin_amdgcn_raw_buffer_load_b16(rb2, lane < 16 ? (80 * jj + 64 + lane) * 2 : 0x80000000u, 0, 0);
                BAR();
                stat_finish<1, false>(sr, lds0, lane);
                f32x4 sq = {0.f, 0.f, 0.f, 0.f};
#pragma unroll
                for (int i = 0; i < 10; ++i) sq += __builtin_bit_cast(f32x4, cq[i]);
#pragma unroll
                for (int r = 0; r < 4; ++r) sq[r] += __shfl_xor(sq[r], 32);
                if (lane < 32) lds_st<f32x4>(lds0 + L_CSQ + qd * 16, sq);
                lds_st<float>(lds0 + L_B1 + lane * 4, bflo(bb0)); lds_st<float>(lds0 + L_B1 + 256 + lane * 4, bflo(bb1));
                lds_st<float>(lds0 + L_B2 + lane * 4, bflo(c0)); lds_st<float>(lds0 + L_B2 + 256 + lane * 4, bflo(c1b));
            } else {
                stat_load<8>(sr, rs, WS_ST + 2 * 131072 + c * 8192, lane);
                BAR();
                stat_check<8>(sr, rs, WS_ST + 2 * 131072 + c * 8192, lane, dead);
                stat_finish<8, false>(sr, lds0, lane);
            }
            if (l == 0) poison_range(rs, vl, WS_KV + (256 + c * 16 + j) * 4 * KVG, 0, 1, 22);
            BAR();
            if (l == 0) {
#pragma unroll
                for (int i = 0; i < 11; ++i) fill_x(rs, vl, WS_X1, WS_ST + 131072, c, j, i);
            }
            BAR();
            WAITV0();
            LBAR();
        } else {
            const u32 src = WS_PQ + j * 163840;
            if (l == 0) { WAITV0(); BAR(); dma_range<PRE_Q>(rs, vl, src, lds0, dw); }
            WAITV((PRE_Q - 48) / 3); BAR();
            dma_range<144 - PRE_Q>(rs, vl, src + PRE_Q * 1024, lds0 + PRE_Q * 1024, dw);
            WAITV((144 - PRE_Q) / 3); BAR();
            dma_range<16>(rs, vl, src + 144 * 1024, lds0, dw);
            WAITV(5); BAR();
            WAITV(0); BAR();
            BAR();
        }
        }
        { PHASE_VARS();
            LBAR();
            const int hd = wid >> 2, w = hd ? 3 - (wid & 3) : wid;
            u32x4 qf[2];
            qf[0] = lds_ld<u32x4>(lds0 + L_Q + w * 4096 + hd * 2048 + vl);
            qf[1] = lds_ld<u32x4>(lds0 + L_Q + w * 4096 + hd * 2048 + 1024 + vl);
            const u32 kv0 = lds0 + L_KV + vl + hd * 1024, kd = 2048 + hd * 256 + t * 16 - vl - hd * 1024;
            const int pc = c - 2 + hd;
            const bool fetch = pc >= 0 && w < 2;
            const u32 ksrc = rfl(WS_KV + (l * 256 + pc * 16 + j) * 4 * KVG + 2 * w * KVG);
            const u32 kdst = rfl(lds0 + L_KV + (4 * hd + 2 * w) * KVG);
            if (fetch) {
#pragma unroll
                for (int i = 0; i < 11; ++i) dma16(rs, vl, ksrc + i * 1024, kdst + i * 1024);
            }
            Att at;
#pragma unroll
            for (int q = 0; q < 2; ++q) { at.nm[q] = f32x4{0.f, 0.f, 0.f, 0.f}; at.o[q] = at.nm[q]; at.orr[q] = at.nm[q]; }
            att_chunk<1, true>(kv0 + (8 + w) * KVG, kv0 + 8 * KVG, w, hd, kd, qf, at, t, g);
            if (fetch) {
                for (int it = 0; it < (1 << 19); ++it) {
                    WAITV(0);
                    u32 m = 0;
#pragma unroll
                    for (int i = 0; i < 11; ++i) m = umax4(m, lds_ld<u32x4>(kdst + vl + i * 1024));
                    if (dead || __builtin_amdgcn_ballot_w64(m == POISON) == 0) break;
                    if (it == (1 << 19) - 1) dead = 1;
                    asm volatile("s_waitcnt lgkmcnt(0)" ::: "memory");
#pragma unroll
                    for (int i = 0; i < 11; ++i) dma16(rs, vl, ksrc + i * 1024, kdst + i * 1024);
                }
            }
            if (w == 2) {
#pragma unroll
                for (int i = 0; i < 25; ++i) dma16(rs, vl, WS_PO + jj * 102400 + (hd * 25 + i) * 1024, lds0 + L_WOA + (hd * 25 + i) * 1024);
            }
            LBAR();
            if (w == 3) {
#pragma unroll
                for (int i = 0; i < 25; ++i)
                    dma16(rs, vl, WS_PO + jj * 102400 + (50 + hd * 25 + i) * 1024,
                          lds0 + (i >= 19 ? (hd ? L_WOC - 19 * 1024 : L_WOB) : L_WOB + hd * 25600) + i * 1024);
            }
            if (c > 0) {
                if (c >= 2) att_chunk<2, false>(kv0 + w * KVG, kv0 + (w + 1) * KVG, 7 - w, hd, kd, qf, at, t, g);
                else att_chunk<0, false>(kv0 + 4 * KVG, kv0 + 5 * KVG, 3, hd, kd, qf, at, t, g);
            }
            const f32x4 (&o)[2] = at.o; const f32x4 (&orr)[2] = at.orr;
            const u32 ob = WS_O + (l * 64 + 4 * c + w) * XT;
            u32x2 po[2], pr[2];
#pragma unroll
            for (int q = 0; q < 2; ++q) {
                auto a = __builtin_amdgcn_permlane32_swap(u_of(orr[q][0]), u_of(orr[q][0]), false, false);
                auto bb = __builtin_amdgcn_permlane16_swap((u32)a[1], (u32)a[1], false, false);
                const float inv = 1.f / f_of(bb[0]);
                po[q] = pk4(o[q] * inv); pr[q] = pk4(orr[q] * inv);
            }
            WAITV(0);
            st16(rs, vl, ob + (5 * j + 2 * hd) * 512, swap_pair(po[0], po[1]));
            if (g == hd) {
                st8(rs, t * 16 + hd * 8, ob + (5 * j + 4) * 512, pr[0]);
                st8(rs, t * 16 + hd * 8 + 256, ob + (5 * j + 4) * 512, pr[1]);
            }
            asm volatile("" ::: "memory"); SB();
#pragma unroll
            for (int k = 0; k < 4; ++k) fill_h(rs, vl, WS_H + l * 64 * HT, c, j, wid + 8 * k);
            WAITV(4);
            LBAR();
        }
        { PHASE_VARS();
        if (wid_o < 4) {
            const int kh = wid >> 1, tile = rfl(4 * c + 2 * hf + (wid & 1));
            u32x2 res[5];
            res_load(res, xin, rs, tile, j, wid, lane);
            f32x4 acc[5][2];
            const u32 vx = g * 512 + t * 16;
            u32x4 xr[10][2];
            gemm_main<5, 10, 5, WoW, PD_O, false>(acc, xr, rs, vx + kh * 256, vx + (kh ^ 1) * 256, WS_O + (l * 64 + tile) * XT + kh * 10 * 2048,
                                     lds0 + vl + L_WOA + kh * 5120, lds0 + vl + L_WOB + kh * 25600,
                                     lds0 + vl + (kh ? L_WOC - 19 * 1024 : L_WOB), dead);
            WAITV0();
            down_epilogue(acc, res, L_EXO, false, false, WS_X1 + l * 64 * XT, WS_ST + (1 + 2 * l) * 131072, rs, lds0, out, c, j, tile, wid, lane);
        } else if (wid_o == 4) {
            if (l == 0) {
                const int qd = lane & 31, hs = lane >> 5;
                u32x4 c1[10];
#pragma unroll
                for (int i = 0; i < 10; ++i) c1[i] = ld16(rs, qd < 28 ? (hs * 10 + i) * 512 + qd * 16 : 0x80000000u, WS_CS1 + j * 10240);
                f32x4 s1 = {0.f, 0.f, 0.f, 0.f};
#pragma unroll
                for (int i = 0; i < 10; ++i) s1 += __builtin_bit_cast(f32x4, c1[i]);
#pragma unroll
                for (int r = 0; r < 4; ++r) s1[r] += __shfl_xor(s1[r], 32);
                if (lane < 32) lds_st<f32x4>(lds0 + L_CS1 + qd * 16, s1);
                __builtin_amdgcn_s_sleep(PZ_SLEEP);
#pragma unroll
                for (int i = 0; i < 11; ++i) fill_x(rs, vl, WS_X0 + 64 * XT, WS_ST + 2 * 131072, c, j, i);
            }
            WAITV0();
            asm volatile("s_waitcnt lgkmcnt(0)" ::: "memory");
            BAR(); BAR();
        } else {
            dma_range<36>(rs, vl, WS_P1 + j * 143360, lds0, dw);
            WAITV(0);
            BAR(); BAR();
            dma_range<PRE_1 - 36>(rs, vl, WS_P1 + j * 143360 + 36 * 1024, lds0 + 36 * 1024, dw);
        }
        }
        { PHASE_VARS();
        if (wid_o < 4) {
            f32x4 acc[7][2];
            const u32 vx = g * 512 + t * 16;
            u32x4 xr[20][2];
            gemm_main<7, 20, 7, W1W, PD_1, false>(acc, xr, rs, vx, vx + 256, WS_X1 + (l * 64 + 4 * c + wid) * XT, lds0 + vl, lds0 + vl + 65536, lds0 + vl + 131072, dead);
            BAR();
            const int kh = wid >> 1, tile = rfl(4 * c + 2 * hf + (wid & 1));
            const u32 vy0 = vx + kh * 256, vy1 = vx + (kh ^ 1) * 256, xso2 = WS_H + (l * 64 + tile) * HT + kh * 27 * 2048;
            u32x2 res[5];
            u32x4 xh[27][2];
            res_load(res, WS_X1 + l * 64 * XT, rs, tile, j, wid, lane);
            gelu_all(acc, rs, lds0, l, c, j, wid, lane, [&]() { gemm_preload<27, W2W, PD_2>(xh, rs, vy0, vy1, xso2); });
            {
            f32x4 acc[5][2];
            gemm_main<5, 27, 5, W2W, PD_2, true>(acc, xh, rs, vy0, vy1, xso2,
                                     lds0 + vl + kh * 5120, lds0 + vl + kh * 5120 + 65536, lds0 + vl + kh * 5120 + 131072, dead);
            down_epilogue(acc, res, L_EX2, true, l == 1, WS_X0 + 64 * XT, WS_ST + 2 * 131072, rs, lds0, out, c, j, tile, wid, lane);
            }
        } else if (wid_o == 4) {
            StatRegs sr;
            BAR();
            stat_load<8>(sr, rs, WS_ST + (1 + 2 * l) * 131072 + c * 8192, lane);
            stat_check<8>(sr, rs, WS_ST + (1 + 2 * l) * 131072 + c * 8192, lane, dead);
            stat_finish<8, true>(sr, lds0, lane);
            BAR();
            LBAR();
            sfor<0, 9>([&](auto M) { constexpr int m = M.v;
                BAR();
                if (l == 0 && m < 8) {
#pragma unroll
                    for (int i = 3 * m; i < 3 * m + 3; ++i) {
                        if (i < 12) fill_o(rs, vl, WS_O + 64 * XT, c, j, i);
                        else if (i < 23) fill_x(rs, vl, WS_X1 + 64 * XT, WS_ST + 3 * 131072, c, j, i - 12);
                    }
                }
            });
            WAITV0();
            BAR(); BAR();
        } else {
            const u32 src = WS_P1 + j * 143360, src2 = WS_P2 + jj * 276480;
            WAITV(0); BAR();
            dma_range<140 - PRE_1>(rs, vl, src + PRE_1 * 1024, lds0 + PRE_1 * 1024, dw);
            WAITV(0); BAR();
            u32x4 stg[40];
            sfor<0, 40>([&](auto I) { constexpr int i = I.v; gload<0>(stg[i], rs, vl, src2 + (150 + 3 * i) * 1024 + dw * 1024); });
            BAR();
            dma_range<150>(rs, vl, src2, lds0, dw);
            WAITV(40); BAR();
            WAITV(30); BAR();
            const u32 sa0 = lds0 + vl + dw * 1024, sa1 = sa0 + 61440;
            sfor<2, 6>([&](auto M) { constexpr int m = M.v;
                if constexpr (m < 5) WAITV(10 * (4 - m));
                asm volatile("s_waitcnt lgkmcnt(0)\n\ts_barrier" ::: "memory");
                sfor<0, 10>([&](auto I) { constexpr int i = I.v;
                    lwrite<((m - 2) & 1) * 30720 + i * 3072>(m < 4 ? sa0 : sa1, stg[(m - 2) * 10 + i]);
                });
            });
            LBAR();
            BAR();
            const u32 srcq = WS_PQ + j * 163840;
            if (l == 0) dma_range<30>(rs, vl, srcq, lds0, dw);
            BAR();
            if (l == 0) dma_range<30>(rs, vl, srcq + 30 * 1024, lds0 + 30 * 1024, dw);
            BAR(); BAR();
            if (l == 0) dma_range<PRE_Q - 60>(rs, vl, srcq + 60 * 1024, lds0 + 60 * 1024, dw);
        }
        }
    }
    WAITV(0);
    if (b == 0 && tid == 256) put_flag(rs, WS_E, tag);
    asm volatile(".Lcode_last:");
}

extern "C" void kernel_launch(void* const* d_in, const int* in_sizes, int n_in, void* d_out, int out_size, void* d_ws, size_t ws_size,
                              hipStream_t stream) {
    (void)in_sizes; (void)n_in; (void)out_size; (void)ws_size;
    (void)hipFuncSetAttribute((const void*)fused_layers, hipFuncAttributeMaxDynamicSharedMemorySize, LDS_BYTES);
    fused_layers<<<256, 512, LDS_BYTES, stream>>>((const u16*)d_in[0], (const u16*)d_in[1], (const u16*)d_in[2], (const u16*)d_in[4],
                                                  (const u16*)d_in[5], (const u16*)d_in[6], (const u16*)d_in[7], (u16*)d_out,
                                                  (unsigned char*)d_ws);
}
```
